```python
import torch

def _softmax(x, axis=-1):
    return torch.softmax(x, dim=axis)
def _gelu(x):
    return torch.nn.functional.gelu(x, approximate="tanh")
def _silu(x):
    return torch.nn.functional.silu(x)
def _sigmoid(x):
    return torch.sigmoid(x)
def _layernorm(x, axis=-1, eps=1e-5):
    m = x.mean(dim=axis, keepdim=True)
    v = x.var(dim=axis, keepdim=True, unbiased=False)
    return (x - m) / torch.sqrt(v + eps)
def _scatter_add(tgt, idx, src, axis=0):
    return tgt.clone().index_add_(axis, idx, src)
def _segment_sum(src, seg, n):
    out = torch.zeros((n,) + src.shape[1:], dtype=src.dtype)
    return out.index_add_(0, seg, src)
def _pad(x, pw):
    flat = tuple(v for pair in reversed(pw) for v in pair)
    return torch.nn.functional.pad(x, flat)

kernel_name = 'sequential_preLN__mha_block__moe_arch_mod__prompt'

def setup_inputs():

    _w = (lambda a: (lambda t: t.to(torch.bfloat16) if t.is_floating_point() else t)(torch.as_tensor(a)))
    def _A_setup():
        B, S, D, H = 1, 2048, 512, 32
        return {
            "x": _w((lambda *s: torch.randn(*s, dtype=torch.bfloat16) * 0.05)(B, S, D)),
            "wqkv": _w((lambda *s: torch.randn(*s, dtype=torch.bfloat16) * 0.05)(D, 3 * D)),
            "wo": _w((lambda *s: torch.randn(*s, dtype=torch.bfloat16) * 0.05)(D, D)),
        }
    _A_in = _A_setup()

    def _B_setup():
        B, S, D, F, C = 1, 2048, 512, 1792, 1
        return {
            "x": _w((lambda *s: torch.randn(*s, dtype=torch.bfloat16) * 0.05)(B, S, D)),
            "w_r": _w((lambda *s: torch.randn(*s, dtype=torch.bfloat16) * 0.05)(D)),
            "w_gate": _w((lambda *s: torch.randn(*s, dtype=torch.bfloat16) * 0.05)(D, F)),
            "w_up": _w((lambda *s: torch.randn(*s, dtype=torch.bfloat16) * 0.05)(D, F)),
            "w_down": _w((lambda *s: torch.randn(*s, dtype=torch.bfloat16) * 0.05)(F, D)),
        }
    _B_in = _B_setup()

    return {
        "A_x": _A_in["x"],
        "A_wqkv": _A_in["wqkv"],
        "A_wo": _A_in["wo"],
        "B_x": _B_in["x"],
        "B_w_r": _B_in["w_r"],
        "B_w_gate": _B_in["w_gate"],
        "B_w_up": _B_in["w_up"],
        "B_w_down": _B_in["w_down"],
    }

def reference(A_x, A_wqkv, A_wo, B_x, B_w_r, B_w_gate, B_w_up, B_w_down):
    def _A_ref(x, wqkv, wo):
        B, S, D = x.shape
        H = 32
        Dh = D // H
        qkv = (lambda x, *d: x.permute(*d))((x @ wqkv).reshape(B, S, 3, H, Dh), 2, 0, 3, 1, 4)
        q, k, v = qkv[0], qkv[1], qkv[2]
        scores = torch.einsum("bhqd,bhkd->bhqk", q, k) * (Dh ** -0.5)
        p = _softmax(scores, axis=-1)
        o = torch.einsum("bhqk,bhkd->bhqd", p, v)
        return (lambda x, *d: x.permute(*d))(o, 0, 2, 1, 3).reshape(B, S, D) @ wo

    def _B_ref(x, w_r, w_gate, w_up, w_down):
        B, S, D = x.shape
        C = 1
        r = x @ w_r
        idx = torch.argsort(-r, axis=-1)[:, :C]
        idx_e = (lambda x, n, axis: torch.repeat_interleave(x, n, dim=axis))(idx.reshape(B, C, 1), D, axis=2)
        x_sel = (lambda x, i, axis: torch.take_along_dim(x, i, dim=axis))(x, idx_e, axis=1)
        g = _silu(x_sel @ w_gate)
        u = x_sel @ w_up
        y = (g * u) @ w_down
        r_sel = _sigmoid((lambda x, i, axis: torch.take_along_dim(x, i, dim=axis))(r, idx, axis=1))
        y = y * r_sel.reshape(B, C, 1)
        b_off = (torch.arange(B) * S).reshape(B, 1)
        flat_idx = (idx + b_off).reshape(B * C)
        zero = (x * 0.0).reshape(B * S, D)
        upd = _scatter_add(zero, flat_idx, y.reshape(B * C, D), axis=0)
        return x + upd.reshape(B, S, D)

    x = A_x
    h = x + _A_ref(_layernorm(x, axis=-1), A_wqkv, A_wo)
    h2 = h + _B_ref(_layernorm(h, axis=-1), B_w_r, B_w_gate, B_w_up, B_w_down)
    return h2
```

```cpp
#include <hip/hip_runtime.h>
#include <hip/hip_bf16.h>
#include <stdint.h>

typedef float f32x4 __attribute__((ext_vector_type(4)));
typedef float f32x8 __attribute__((ext_vector_type(8)));
typedef float f32x16 __attribute__((ext_vector_type(16)));
typedef __bf16 bf16x2 __attribute__((ext_vector_type(2)));
typedef __bf16 bf16x8 __attribute__((ext_vector_type(8)));
typedef __bf16 bf16x16 __attribute__((ext_vector_type(16)));
typedef short s16x4 __attribute__((ext_vector_type(4)));
typedef unsigned u32x2 __attribute__((ext_vector_type(2)));
typedef unsigned u32x4 __attribute__((ext_vector_type(4)));
typedef unsigned long long u64;
#define GAS __attribute__((address_space(1)))
#define LAS __attribute__((address_space(3)))
#define RLX_AGENT __ATOMIC_RELAXED, __HIP_MEMORY_SCOPE_AGENT
#define SBAR() __builtin_amdgcn_sched_barrier(0)

constexpr int SEQ = 2048, DM = 512, NH = 32, FF = 1792;
constexpr size_t WS_Q = 0, WS_K = 2u << 20, WS_V = 4u << 20, WS_O = 6u << 20, WS_ST = 8u << 20;
constexpr size_t WS_CKEY = 9u << 20, WS_CLN = WS_CKEY + 4096, WS_CH = WS_CLN + 128 * 1024, WS_ACT = WS_CH + 128 * 1024;
constexpr float QSCALE = 0.25f * 1.4426950408889634f;

__device__ __forceinline__ unsigned pk_bf16(float lo, float hi) { bf16x2 v; v[0] = (__bf16)lo; v[1] = (__bf16)hi; return __builtin_bit_cast(unsigned, v); }
__device__ __forceinline__ float bf_lo(unsigned u) { return __uint_as_float(u << 16); }
__device__ __forceinline__ float bf_hi(unsigned u) { return __uint_as_float(u & 0xffff0000u); }
__device__ __forceinline__ float rbf(float x) { return (float)(__bf16)x; }
__device__ __forceinline__ void glds16(const void* g, unsigned lds_base) {
    unsigned sv; asm volatile("s_mov_b32 %0, m0\n\ts_mov_b32 m0, %2\n\ts_nop 0\n\tglobal_load_lds_dwordx4 %1, off\n\ts_mov_b32 m0, %0" : "=&s"(sv) : "v"(g), "s"(lds_base) : "memory");
}
typedef short v4i16_t __attribute__((ext_vector_type(4)));
__device__ __forceinline__ u32x2 tr_read(LAS const char* p) { return __builtin_bit_cast(u32x2, __builtin_amdgcn_ds_read_tr16_b64_v4i16((LAS v4i16_t*)p)); }
__device__ __forceinline__ float wave_sum(float v) {
#pragma unroll
    for (int o = 32; o >= 1; o >>= 1) v += __shfl_xor(v, o);
    return v;
}
#define MFMA16(a, b, c) __builtin_amdgcn_mfma_f32_16x16x32_bf16(__builtin_bit_cast(bf16x8, a), __builtin_bit_cast(bf16x8, b), c, 0, 0, 0)
#define MFMA32(a, b, c) __builtin_amdgcn_mfma_f32_32x32x16_bf16(__builtin_bit_cast(bf16x8, a), __builtin_bit_cast(bf16x8, b), c, 0, 0, 0)

__global__ __launch_bounds__(512, 2) void k_qkv(const unsigned short* __restrict__ x, const unsigned short* __restrict__ wqkv, char* __restrict__ ws) {
    extern __shared__ __attribute__((aligned(16))) char lds[];
    const int g = blockIdx.x & 7, rb = blockIdx.x >> 3;
    const int tid = threadIdx.x, l = tid & 63, w = __builtin_amdgcn_readfirstlane(tid >> 6);
    const unsigned lds0 = (unsigned)(uintptr_t)lds;
    if (tid < 192) ((u64*)(ws + WS_ST))[blockIdx.x * 192 + tid] = 0ull;
    u32x4 xr[8];
#pragma unroll
    for (int i = 0; i < 8; ++i) xr[i] = *(const u32x4*)(x + (size_t)(64 * rb + 8 * w + i) * DM + l * 8);
    auto dma_w = [&](int hg, unsigned slot_off) {
        const int colbase = (hg < 2 ? 512 : hg < 4 ? 1024 : 0) + 64 * g, kbase = 256 * (hg & 1);
#pragma unroll
        for (int i = 0; i < 4; ++i) {
            const int ins = 4 * w + i, row = 8 * ins + (l >> 3);
            const int f = ((((l >> 4) & 1) | ((i & 1) << 1)) << 1);
            glds16(wqkv + (size_t)(kbase + row) * (3 * DM) + colbase + ((l & 7) ^ f) * 8, lds0 + slot_off + ins * 1024);
        }
    };
    constexpr unsigned A_OFF = 0, W_OFF = 65536;
    dma_w(0, W_OFF); dma_w(1, W_OFF + 32768); dma_w(2, W_OFF + 65536);
#pragma unroll
    for (int i = 0; i < 8; ++i) {
        float v[8];
#pragma unroll
        for (int j = 0; j < 4; ++j) { v[2 * j] = bf_lo(xr[i][j]); v[2 * j + 1] = bf_hi(xr[i][j]); }
        float s1 = 0.f, s2 = 0.f;
#pragma unroll
        for (int j = 0; j < 8; ++j) { s1 += v[j]; s2 += v[j] * v[j]; }
        s1 = wave_sum(s1); s2 = wave_sum(s2);
        const float m = s1 * (1.f / DM); const float var = fmaxf(s2 * (1.f / DM) - m * m, 0.f);
        const float mb = rbf(m), sb = rbf(sqrtf(rbf(rbf(var) + 1e-5f))), rs = 1.f / sb;
        u32x4 o;
#pragma unroll
        for (int j = 0; j < 4; ++j) o[j] = pk_bf16((v[2 * j] - mb) * rs, (v[2 * j + 1] - mb) * rs);
        const int row = 8 * w + i;
        *(u32x4*)(lds + A_OFF + row * 1024 + ((l & 48) | ((l & 15) ^ (row & 15))) * 16) = o;
    }
    asm volatile("s_waitcnt lgkmcnt(0)\n\ts_barrier" ::: "memory");
    const int rh = w >> 2, hh = w & 3, c = l & 15, g4 = l >> 4;
    u32x4 af[2][16];
#pragma unroll
    for (int t = 0; t < 2; ++t)
#pragma unroll
        for (int s = 0; s < 16; ++s) {
            const int row = 32 * rh + 16 * t + c, ch = 4 * s + g4;
            af[t][s] = *(const u32x4*)(lds + A_OFF + row * 1024 + ((ch & 48) | ((ch & 15) ^ c)) * 16);
        }
    asm volatile("s_waitcnt vmcnt(0) lgkmcnt(0)\n\ts_barrier" ::: "memory");
    dma_w(3, A_OFF); dma_w(4, A_OFF + 32768);
    const int q = (l & 15) >> 2, p = l & 3;
    const int brow = 8 * g4 + q;
    const int bf = ((((brow >> 1) & 1) | (((brow >> 3) & 1) << 1)) << 1);
    const unsigned boff = brow * 128 + (((2 * hh + (p >> 1)) ^ bf) * 16) + (p & 1) * 8;
    const int head = 4 * g + hh;
    auto gemm_half = [&](unsigned slot_off, int kh, f32x4& a0, f32x4& a1, bool vmode) {
        LAS const char* bp = (LAS const char*)lds + slot_off + boff;
#pragma unroll
        for (int s = 0; s < 8; ++s) {
            u32x2 b0 = tr_read(bp + s * 4096), b1 = tr_read(bp + s * 4096 + 512);
            u32x4 b = {b0[0], b0[1], b1[0], b1[1]};
            if (vmode) { a0 = MFMA16(af[0][8 * kh + s], b, a0); a1 = MFMA16(af[1][8 * kh + s], b, a1); }
            else       { a0 = MFMA16(b, af[0][8 * kh + s], a0); a1 = MFMA16(b, af[1][8 * kh + s], a1); }
        }
    };
    const size_t tile_off = (size_t)head * 65536 + (size_t)(2 * rb + rh) * 1024;
    {
        f32x4 a0 = {}, a1 = {};
        gemm_half(W_OFF, 0, a0, a1, false);
        asm volatile("s_waitcnt lgkmcnt(0)\n\ts_barrier" ::: "memory");
        dma_w(5, W_OFF);
        gemm_half(W_OFF + 32768, 1, a0, a1, false);
        char* kp = ws + WS_K + tile_off + (g4 >> 1) * 512 + c * 16 + (g4 & 1) * 8;
        *(u32x2*)(kp) = (u32x2){pk_bf16(a0[0], a0[1]), pk_bf16(a0[2], a0[3])};
        *(u32x2*)(kp + 256) = (u32x2){pk_bf16(a1[0], a1[1]), pk_bf16(a1[2], a1[3])};
    }
    {
        f32x4 a0 = {}, a1 = {};
        gemm_half(W_OFF + 65536, 0, a0, a1, true);
        asm volatile("s_waitcnt vmcnt(0)\n\ts_barrier" ::: "memory");
        gemm_half(A_OFF, 1, a0, a1, true);
        char* vp = ws + WS_V + tile_off + (g4 >> 1) * 256 + c * 16 + (g4 & 1) * 8;
        *(u32x2*)(vp) = (u32x2){pk_bf16(a0[0], a0[1]), pk_bf16(a0[2], a0[3])};
        *(u32x2*)(vp + 512) = (u32x2){pk_bf16(a1[0], a1[1]), pk_bf16(a1[2], a1[3])};
    }
    {
        f32x4 a0 = {}, a1 = {};
        gemm_half(A_OFF + 32768, 0, a0, a1, false);
        gemm_half(W_OFF, 1, a0, a1, false);
        char* qp = ws + WS_Q + tile_off + (g4 >> 1) * 512 + c * 16 + (g4 & 1) * 8;
        *(u32x2*)(qp) = (u32x2){pk_bf16(a0[0] * QSCALE, a0[1] * QSCALE), pk_bf16(a0[2] * QSCALE, a0[3] * QSCALE)};
        *(u32x2*)(qp + 256) = (u32x2){pk_bf16(a1[0] * QSCALE, a1[1] * QSCALE), pk_bf16(a1[2] * QSCALE, a1[3] * QSCALE)};
    }
}

#define SMF(v, p, o) __builtin_amdgcn_smfmac_f32_32x32x32_bf16(__builtin_bit_cast(bf16x8, v), __builtin_bit_cast(bf16x16, p), o, sidx, 0, 0)
#define LO8(s) __builtin_shufflevector(s, s, 0, 1, 2, 3, 4, 5, 6, 7)
#define HI8(s) __builtin_shufflevector(s, s, 8, 9, 10, 11, 12, 13, 14, 15)
#define EX(s, i) s[i] = __builtin_amdgcn_exp2f(s[i])
#define PIN(x) asm volatile("" : "+v"(x))

__device__ __forceinline__ float max16(const f32x16& s) {
    float a = fmaxf(fmaxf(s[0], s[1]), s[2]), b = fmaxf(fmaxf(s[3], s[4]), s[5]);
    a = fmaxf(fmaxf(a, s[6]), s[7]); b = fmaxf(fmaxf(b, s[8]), s[9]);
    a = fmaxf(fmaxf(a, s[10]), s[11]); b = fmaxf(fmaxf(b, s[12]), s[13]);
    a = fmaxf(fmaxf(a, s[14]), s[15]);
    return fmaxf(a, b);
}
__device__ __forceinline__ float half_max(float m) {
    auto rr = __builtin_amdgcn_permlane32_swap(__float_as_uint(m), __float_as_uint(m), false, false);
    return fmaxf(__uint_as_float(rr[0]), __uint_as_float(rr[1]));
}

__global__ __launch_bounds__(512, 2) void k_attn(char* __restrict__ ws) {
    __shared__ __attribute__((aligned(16))) float xch[8 * 10 * 64];
    const int g = blockIdx.x & 7, rb = blockIdx.x >> 3;
    const int tid = threadIdx.x, l = tid & 63, w = __builtin_amdgcn_readfirstlane(tid >> 6);
    const int hh = w & 3, kh = w >> 2, head = 4 * g + hh;
    const char* qb = ws + WS_Q + (size_t)head * 65536 + (size_t)rb * 2048;
    const u32x4 q0 = *(const u32x4*)(qb + l * 16), q1 = *(const u32x4*)(qb + 1024 + l * 16);
    const char* kbase = ws + WS_K + (size_t)head * 65536 + (size_t)kh * 32768;
    const char* vbase = ws + WS_V + (size_t)head * 65536 + (size_t)kh * 32768;
    const unsigned koff = l * 16, voff = (l >> 5) * 256 + (l & 15) * 16;
    const u64 vmask = 0x0000FFFF0000FFFFull;
    const int sidx = 0xDDDD;
    f32x16 o0, o1; float m0 = -INFINITY, m1 = -INFINITY;
    const f32x16 zero16 = {};
    for (int attempt = 0; attempt < 2; ++attempt) {
        const int nt = attempt ? 32 : 1;
        for (int t = 0; t < nt; ++t) {
            const u32x4 kf = *(const u32x4*)(kbase + t * 1024 + koff);
            f32x16 s0 = MFMA32(kf, q0, zero16), s1 = MFMA32(kf, q1, zero16);
            m0 = fmaxf(m0, max16(s0)); m1 = fmaxf(m1, max16(s1));
        }
        m0 = half_max(m0); m1 = half_max(m1);
        f32x16 n0, n1;
#pragma unroll
        for (int i = 0; i < 16; ++i) { n0[i] = -m0; n1[i] = -m1; }
        PIN(n0); PIN(n1);
        o0 = zero16; o1 = zero16;
        u32x4 kf0, kf1, kf2, kf3, va0, vb0, va1, vb1, va2, vb2, va3, vb3;
        const u32x4 ones = {0x3F803F80u, 0x3F803F80u, 0x3F803F80u, 0x3F803F80u};
        va0 = vb0 = va1 = vb1 = va2 = vb2 = va3 = vb3 = ones;
        const char* kp = kbase; const char* vp = vbase;
#define LDK(kf, off) asm volatile("global_load_dwordx4 %0, %1, %2 offset:" #off : "=v"(kf) : "v"(koff), "s"(kp) : "memory")
#define LDV(va, vb, offa, offb) asm volatile("s_mov_b64 exec, %4\n\tglobal_load_dwordx4 %0, %2, %3 offset:" #offa "\n\tglobal_load_dwordx4 %1, %2, %3 offset:" #offb "\n\ts_mov_b64 exec, -1" : "+v"(va), "+v"(vb) : "v"(voff), "s"(vp), "s"(vmask) : "memory")
        asm volatile("s_waitcnt vmcnt(0)" ::: "memory");
        LDK(kf0, 0); LDV(va0, vb0, 0, 512); LDK(kf1, 1024); LDV(va1, vb1, 1024, 1536);
        LDK(kf2, 2048); LDV(va2, vb2, 2048, 2560); LDK(kf3, 3072);
        asm volatile("s_waitcnt vmcnt(9)" : "+v"(kf0) :: "memory");
        f32x16 s0 = MFMA32(kf0, q0, n0), s1;
#define STEP(KC, KN, VA, VB, VPA, VPB, KOFF, VOFFA, VOFFB) do { \
            SBAR(); \
            s1 = MFMA32(KC, q1, n1); SBAR(); \
            LDV(VPA, VPB, VOFFA, VOFFB); LDK(KC, KOFF); \
            asm volatile("s_waitcnt vmcnt(9)" : "+v"(VA), "+v"(VB), "+v"(KN) :: "memory"); SBAR(); \
            EX(s0, 0); EX(s0, 1); EX(s0, 2); EX(s0, 3); EX(s0, 4); EX(s0, 5); EX(s0, 6); EX(s0, 7); EX(s0, 8); EX(s0, 9); PIN(s0); SBAR(); \
            o0 = SMF(VA, LO8(s0), o0); SBAR(); \
            EX(s0, 10); EX(s0, 11); EX(s0, 12); EX(s0, 13); EX(s0, 14); EX(s0, 15); PIN(s0); EX(s1, 0); EX(s1, 1); PIN(s1); SBAR(); \
            o0 = SMF(VB, HI8(s0), o0); SBAR(); \
            s0 = MFMA32(KN, q0, n0); SBAR(); \
            EX(s1, 2); EX(s1, 3); EX(s1, 4); EX(s1, 5); EX(s1, 6); EX(s1, 7); EX(s1, 8); EX(s1, 9); PIN(s1); SBAR(); \
            o1 = SMF(VA, LO8(s1), o1); SBAR(); \
            EX(s1, 10); EX(s1, 11); EX(s1, 12); EX(s1, 13); EX(s1, 14); EX(s1, 15); PIN(s1); SBAR(); \
            o1 = SMF(VB, HI8(s1), o1); SBAR(); \
        } while (0)
        for (int it = 0; it < 8; ++it) {
            const char* kpn = kp + 4096; const char* vpn = vp + 4096;
            { const char* kp = kpn; STEP(kf0, kf1, va0, vb0, va3, vb3, 0, 3072, 3584); }
            kp = kpn; vp = vpn;
            STEP(kf1, kf2, va1, vb1, va0, vb0, 1024, 0, 512);
            STEP(kf2, kf3, va2, vb2, va1, vb1, 2048, 1024, 1536);
            STEP(kf3, kf0, va3, vb3, va2, vb2, 3072, 2048, 2560);
        }
        asm volatile("s_waitcnt vmcnt(0)" : "+v"(kf0), "+v"(kf1), "+v"(kf2), "+v"(kf3), "+v"(va0), "+v"(vb0), "+v"(va1), "+v"(vb1), "+v"(va2), "+v"(vb2) :: "memory");
        float mx = 0.f;
#pragma unroll
        for (int i = 0; i < 8; ++i) mx = fmaxf(mx, fmaxf(fabsf(o0[i]), fabsf(o1[i])));
        const bool ok = (o0[8] <= 16777216.f) && (o1[8] <= 16777216.f) && (mx < INFINITY);
        if (__all(ok)) break;
    }
    float* mine = xch + w * 640; const float* other = xch + (w ^ 4) * 640;
    {
        const f32x16& os = kh ? o0 : o1; const float ms = kh ? m0 : m1;
#pragma unroll
        for (int i = 0; i < 9; ++i) mine[i * 64 + l] = os[i];
        mine[9 * 64 + l] = ms;
    }
    __syncthreads();
    {
        const f32x16& ok_ = kh ? o1 : o0; const float mk = kh ? m1 : m0;
        const float mo = other[9 * 64 + l], M = fmaxf(mk, mo);
        const float fa = __builtin_amdgcn_exp2f(mk - M), fb = __builtin_amdgcn_exp2f(mo - M);
        const float lsum = ok_[8] * fa + other[8 * 64 + l] * fb, rl = 1.f / lsum;
        float r[8];
#pragma unroll
        for (int i = 0; i < 8; ++i) r[i] = (ok_[i] * fa + other[i * 64 + l] * fb) * rl;
        u32x4 ov = {pk_bf16(r[0], r[1]), pk_bf16(r[2], r[3]), pk_bf16(r[4], r[5]), pk_bf16(r[6], r[7])};
        *(u32x4*)(ws + WS_O + (size_t)head * 65536 + (size_t)(64 * rb + 32 * kh + (l & 31)) * 32 + (l >> 5) * 16) = ov;
    }
}

__global__ __launch_bounds__(512, 2) void k_oproj(const unsigned short* __restrict__ x, const unsigned short* __restrict__ wo, const unsigned short* __restrict__ wr,
                                                  char* __restrict__ ws, unsigned short* __restrict__ out) {
    extern __shared__ __attribute__((aligned(16))) char lds[];
    const int g = blockIdx.x & 7, rb = blockIdx.x >> 3;
    const int tid = threadIdx.x, l = tid & 63, w = __builtin_amdgcn_readfirstlane(tid >> 6);
    const unsigned lds0 = (unsigned)(uintptr_t)lds;
    const int rq = w & 3, kh = w >> 2, c = l & 15, g4 = l >> 4;
    const int row = 64 * rb + 16 * rq + c;
#pragma unroll
    for (int i = 0; i < 8; ++i) {
        const int ins = 8 * w + i, r = 8 * ins + (l >> 3);
        glds16(wo + (size_t)r * DM + 64 * g + ((l & 7) ^ (((r >> 1) & 3) << 1)) * 8, lds0 + ins * 1024);
    }
    u32x4 of[8];
#pragma unroll
    for (int s = 0; s < 8; ++s) of[s] = *(const u32x4*)(ws + WS_O + (size_t)(2 * (8 * kh + s) + (g4 >> 1)) * 65536 + (size_t)row * 32 + (g4 & 1) * 16);
    u32x2 xv[4], wv[4];
#pragma unroll
    for (int ct = 0; ct < 4; ++ct) { xv[ct] = *(const u32x2*)(x + (size_t)row * DM + 64 * g + 16 * ct + 4 * g4); wv[ct] = *(const u32x2*)(wr + 64 * g + 16 * ct + 4 * g4); }
    const u32x4 wall = *(const u32x4*)(wr + l * 8);
    float wsum = 0.f;
#pragma unroll
    for (int j = 0; j < 4; ++j) wsum += bf_lo(wall[j]) + bf_hi(wall[j]);
    wsum = wave_sum(wsum);
    asm volatile("s_waitcnt vmcnt(0)\n\ts_barrier" ::: "memory");
    const int q = (l & 15) >> 2, p = l & 3;
    const int brow = (g4 >> 1) * 16 + 4 * (g4 & 1) + q;
    f32x4 acc[4] = {};
#pragma unroll
    for (int s = 0; s < 8; ++s) {
#pragma unroll
        for (int ct = 0; ct < 4; ++ct) {
            LAS const char* bp = (LAS const char*)lds + (32 * (8 * kh + s) + brow) * 128 + (((2 * ct + (p >> 1)) ^ (((brow >> 1) & 3) << 1)) * 16) + (p & 1) * 8;
            u32x2 b0 = tr_read(bp), b1 = tr_read(bp + 1024);
            u32x4 b = {b0[0], b0[1], b1[0], b1[1]};
            acc[ct] = MFMA16(b, of[s], acc[ct]);
        }
    }
    f32x4* red = (f32x4*)(lds + 65536);
    if (kh) {
#pragma unroll
        for (int ct = 0; ct < 4; ++ct) red[(rq * 4 + ct) * 64 + l] = acc[ct];
    }
    __syncthreads();
    if (kh) return;
    float h[16]; float s1 = 0.f, s2 = 0.f, s3 = 0.f;
#pragma unroll
    for (int ct = 0; ct < 4; ++ct) {
        const f32x4 o = red[(rq * 4 + ct) * 64 + l];
#pragma unroll
        for (int j = 0; j < 4; ++j) {
            const float a = rbf(acc[ct][j] + o[j]);
            const float xx = (j & 1) ? bf_hi(xv[ct][j >> 1]) : bf_lo(xv[ct][j >> 1]);
            const float ww = (j & 1) ? bf_hi(wv[ct][j >> 1]) : bf_lo(wv[ct][j >> 1]);
            const float hv = rbf(xx + a);
            h[ct * 4 + j] = hv; s1 += hv; s2 += hv * hv; s3 += hv * ww;
        }
    }
    s1 += __shfl_xor(s1, 16); s2 += __shfl_xor(s2, 16); s3 += __shfl_xor(s3, 16);
    s1 += __shfl_xor(s1, 32); s2 += __shfl_xor(s2, 32); s3 += __shfl_xor(s3, 32);
    GAS u64* st = (GAS u64*)(ws + WS_ST) + (size_t)rb * 8 * 192;
    const unsigned tag = 1u;
    if (g4 < 3) {
        const float sv = g4 == 0 ? s1 : g4 == 1 ? s2 : s3;
        __hip_atomic_store(st + g * 192 + g4 * 64 + 16 * rq + c, ((u64)tag << 32) | __float_as_uint(sv), RLX_AGENT);
    }
    float t1, t2, t3;
    for (unsigned spins = 0;; ++spins) {
        u64 v[6]; bool ok = true;
#pragma unroll
        for (int k = 0; k < 6; ++k) { v[k] = __hip_atomic_load(st + (2 * g4 + k / 3) * 192 + (k % 3) * 64 + 16 * rq + c, RLX_AGENT); ok &= (unsigned)(v[k] >> 32) == tag; }
        t1 = __uint_as_float((unsigned)v[0]) + __uint_as_float((unsigned)v[3]);
        t2 = __uint_as_float((unsigned)v[1]) + __uint_as_float((unsigned)v[4]);
        t3 = __uint_as_float((unsigned)v[2]) + __uint_as_float((unsigned)v[5]);
        if (__all(ok) || spins > (1u << 20)) break;
        __builtin_amdgcn_s_sleep(1);
    }
    t1 += __shfl_xor(t1, 16); t2 += __shfl_xor(t2, 16); t3 += __shfl_xor(t3, 16);
    t1 += __shfl_xor(t1, 32); t2 += __shfl_xor(t2, 32); t3 += __shfl_xor(t3, 32);
    const float m = t1 * (1.f / DM), var = fmaxf(t2 * (1.f / DM) - m * m, 0.f);
    const float mb = rbf(m), sb = rbf(sqrtf(rbf(rbf(var) + 1e-5f))), rs = 1.f / sb;
    const float r = (t3 - mb * wsum) * rs;
    float br = r; int bi = c;
#pragma unroll
    for (int o = 1; o <= 8; o <<= 1) { const float orr = __shfl_xor(br, o); const int oi = __shfl_xor(bi, o); if (orr > br || (orr == br && oi < bi)) { br = orr; bi = oi; } }
    const int cand = rb * 4 + rq;
#pragma unroll
    for (int ct = 0; ct < 4; ++ct) {
        float ln[4];
#pragma unroll
        for (int j = 0; j < 4; ++j) ln[j] = rbf(rbf(h[ct * 4 + j] - mb) * rs);
        const int col = 64 * g + 16 * ct + 4 * g4;
        *(u32x2*)(out + (size_t)row * DM + col) = (u32x2){pk_bf16(h[ct * 4] + ln[0], h[ct * 4 + 1] + ln[1]), pk_bf16(h[ct * 4 + 2] + ln[2], h[ct * 4 + 3] + ln[3])};
        if (c == bi) {
            *(u32x2*)(ws + WS_CLN + (size_t)cand * 1024 + col * 2) = (u32x2){pk_bf16(ln[0], ln[1]), pk_bf16(ln[2], ln[3])};
            *(u32x2*)(ws + WS_CH + (size_t)cand * 1024 + col * 2) = (u32x2){pk_bf16(h[ct * 4], h[ct * 4 + 1]), pk_bf16(h[ct * 4 + 2], h[ct * 4 + 3])};
        }
    }
    if (g == 0 && l == 0) *(u32x2*)(ws + WS_CKEY + cand * 8) = (u32x2){__float_as_uint(br), (unsigned)(64 * rb + 16 * rq + bi)};
}

__device__ __forceinline__ void pick_winner(const char* ws, int l, float& br, int& brow, int& bc) {
    const u32x4 kk = *(const u32x4*)(ws + WS_CKEY + l * 16);
    br = __uint_as_float(kk[0]); brow = kk[1]; bc = 2 * l;
    { const float r2 = __uint_as_float(kk[2]); if (r2 > br) { br = r2; brow = kk[3]; bc = 2 * l + 1; } }
#pragma unroll
    for (int o = 1; o <= 32; o <<= 1) {
        const float orr = __shfl_xor(br, o); const int orow = __shfl_xor(brow, o), oc = __shfl_xor(bc, o);
        if (orr > br || (orr == br && orow < brow)) { br = orr; brow = orow; bc = oc; }
    }
}

__global__ __launch_bounds__(256) void k_ffn1(const unsigned short* __restrict__ wg, const unsigned short* __restrict__ wu, char* __restrict__ ws) {
    __shared__ float red[4][2][32];
    const int tid = threadIdx.x, l = tid & 63, w = tid >> 6;
    const int c8 = tid & 3, kg = tid >> 2, col0 = blockIdx.x * 32 + c8 * 8;
    u32x4 gw[8], uw[8];
#pragma unroll
    for (int i = 0; i < 8; ++i) { gw[i] = *(const u32x4*)(wg + (size_t)(kg * 8 + i) * FF + col0); uw[i] = *(const u32x4*)(wu + (size_t)(kg * 8 + i) * FF + col0); }
    float br; int brow, bc; pick_winner(ws, l, br, brow, bc);
    const u32x4 xv = *(const u32x4*)(ws + WS_CLN + (size_t)bc * 1024 + kg * 16);
    float ag[8] = {}, au[8] = {};
#pragma unroll
    for (int i = 0; i < 8; ++i) {
        const float xx = (i & 1) ? bf_hi(xv[i >> 1]) : bf_lo(xv[i >> 1]);
#pragma unroll
        for (int j = 0; j < 4; ++j) { ag[2 * j] += xx * bf_lo(gw[i][j]); ag[2 * j + 1] += xx * bf_hi(gw[i][j]); au[2 * j] += xx * bf_lo(uw[i][j]); au[2 * j + 1] += xx * bf_hi(uw[i][j]); }
    }
#pragma unroll
    for (int j = 0; j < 8; ++j)
#pragma unroll
        for (int o = 4; o <= 32; o <<= 1) { ag[j] += __shfl_xor(ag[j], o); au[j] += __shfl_xor(au[j], o); }
    if (l < 4) {
#pragma unroll
        for (int j = 0; j < 8; ++j) { red[w][0][l * 8 + j] = ag[j]; red[w][1][l * 8 + j] = au[j]; }
    }
    __syncthreads();
    if (tid < 32) {
        const float gg = rbf(red[0][0][tid] + red[1][0][tid] + red[2][0][tid] + red[3][0][tid]);
        const float uu = rbf(red[0][1][tid] + red[1][1][tid] + red[2][1][tid] + red[3][1][tid]);
        const float sg = rbf(gg / (1.f + __expf(-gg)));
        ((float*)(ws + WS_ACT))[blockIdx.x * 32 + tid] = rbf(sg * uu);
    }
}

__global__ __launch_bounds__(256) void k_ffn2(const unsigned short* __restrict__ wd, const char* __restrict__ ws, unsigned short* __restrict__ out) {
    __shared__ float red[4][16];
    const int tid = threadIdx.x, l = tid & 63, w = tid >> 6;
    const int c8 = tid & 1, kg = tid >> 1, col0 = blockIdx.x * 16 + c8 * 8;
    u32x4 dw[14]; float a[14];
#pragma unroll
    for (int i = 0; i < 14; ++i) { dw[i] = *(const u32x4*)(wd + (size_t)(kg * 14 + i) * DM + col0); a[i] = ((const float*)(ws + WS_ACT))[kg * 14 + i]; }
    float br; int brow, bc; pick_winner(ws, l, br, brow, bc);
    float acc[8] = {};
#pragma unroll
    for (int i = 0; i < 14; ++i)
#pragma unroll
        for (int j = 0; j < 4; ++j) { acc[2 * j] += a[i] * bf_lo(dw[i][j]); acc[2 * j + 1] += a[i] * bf_hi(dw[i][j]); }
#pragma unroll
    for (int j = 0; j < 8; ++j)
#pragma unroll
        for (int o = 2; o <= 32; o <<= 1) acc[j] += __shfl_xor(acc[j], o);
    if (l < 2) {
#pragma unroll
        for (int j = 0; j < 8; ++j) red[w][l * 8 + j] = acc[j];
    }
    __syncthreads();
    if (tid < 16) {
        const int col = blockIdx.x * 16 + tid;
        const float y = rbf(red[0][tid] + red[1][tid] + red[2][tid] + red[3][tid]);
        const float rr = rbf(br), sig = rbf(1.f / (1.f + __expf(-rr)));
        const float ys = rbf(y * sig);
        const float ln = bf_lo(((const unsigned short*)(ws + WS_CLN))[bc * 512 + col]), hv = bf_lo(((const unsigned short*)(ws + WS_CH))[bc * 512 + col]);
        const float res = hv + rbf(ln + ys);
        out[(size_t)brow * DM + col] = (unsigned short)(pk_bf16(res, 0.f) & 0xffffu);
    }
}

extern "C" void kernel_launch(void* const* d_in, const int* in_sizes, int n_in, void* d_out, int out_size, void* d_ws, size_t ws_size, hipStream_t stream) {
    const unsigned short* x = (const unsigned short*)d_in[0];
    const unsigned short* wqkv = (const unsigned short*)d_in[1];
    const unsigned short* wo = (const unsigned short*)d_in[2];
    const unsigned short* wr = (const unsigned short*)d_in[4];
    const unsigned short* wg = (const unsigned short*)d_in[5];
    const unsigned short* wu = (const unsigned short*)d_in[6];
    const unsigned short* wd = (const unsigned short*)d_in[7];
    char* ws = (char*)d_ws; unsigned short* out = (unsigned short*)d_out;
    (void)hipFuncSetAttribute((const void*)k_qkv, hipFuncAttributeMaxDynamicSharedMemorySize, 163840);
    (void)hipFuncSetAttribute((const void*)k_oproj, hipFuncAttributeMaxDynamicSharedMemorySize, 81920 + 16384);
    k_qkv<<<256, 512, 163840, stream>>>(x, wqkv, ws);
    k_attn<<<256, 512, 0, stream>>>(ws);
    k_oproj<<<256, 512, 81920 + 16384, stream>>>(x, wo, wr, ws, out);
    k_ffn1<<<56, 256, 0, stream>>>(wg, wu, ws);
    k_ffn2<<<32, 256, 0, stream>>>(wd, ws, out);
}
```

```cpp
#include <hip/hip_runtime.h>
#include <hip/hip_bf16.h>
#include <stdint.h>

typedef float f32x2 __attribute__((ext_vector_type(2)));
typedef float f32x4 __attribute__((ext_vector_type(4)));
typedef float f32x16 __attribute__((ext_vector_type(16)));
typedef __bf16 bf16x2 __attribute__((ext_vector_type(2)));
typedef __bf16 bf16x8 __attribute__((ext_vector_type(8)));
typedef __bf16 bf16x16 __attribute__((ext_vector_type(16)));
typedef unsigned u32x2 __attribute__((ext_vector_type(2)));
typedef unsigned u32x4 __attribute__((ext_vector_type(4)));
typedef unsigned long long u64;
typedef unsigned short u16;
#define LAS __attribute__((address_space(3)))
#define SBAR() __builtin_amdgcn_sched_barrier(0)
#define PIN(x) asm volatile("" : "+v"(x))
#define CFENCE() asm volatile("" ::: "memory")

constexpr int SEQ = 2048, DM = 512, NH = 32, FF = 1792;
constexpr unsigned KV_STRIDE = 131072 + 8192;
constexpr unsigned WS_KV = 0;
constexpr unsigned WS_O = 5u << 20;
constexpr unsigned WS_CTL = 7u << 20;
constexpr unsigned WS_FLAG = WS_CTL;
constexpr unsigned WS_EPOCH = WS_CTL + 32768;
constexpr unsigned WS_CKEY = WS_CTL + 36864;
constexpr unsigned WS_ACT = WS_CTL + 40960;
constexpr unsigned WS_ST = WS_CTL + 49152;
constexpr unsigned WS_CLN = WS_ST + 32 * 4 * 8 * 256;
constexpr unsigned WS_CH = WS_CLN + 131072;
constexpr unsigned WS_END = WS_CH + 131072;
constexpr unsigned SENT = 0xFFFFFFFFu;
constexpr unsigned SPIN_CAP = 1u << 18;
constexpr float QSCALE = 0.25f * 1.4426950408889634f;
constexpr int N_FFN1 = 56, FFN2_BASE = 64, N_FFN2 = 32;
constexpr int LDS_BYTES = 163840;

#define STAMP(i)

__device__ __forceinline__ unsigned pk_bf16(float lo, float hi) { bf16x2 v; v[0] = (__bf16)lo; v[1] = (__bf16)hi; return __builtin_bit_cast(unsigned, v); }
__device__ __forceinline__ float bf_lo(unsigned u) { return __uint_as_float(u << 16); }
__device__ __forceinline__ float bf_hi(unsigned u) { return __uint_as_float(u & 0xffff0000u); }
__device__ __forceinline__ float rbf(float x) { return (float)(__bf16)x; }
__device__ __forceinline__ unsigned nosent(unsigned u) { return u < 0xFFFFFFFEu ? u : 0xFFFFFFFEu; }
__device__ __forceinline__ unsigned umax3(unsigned a, unsigned b, unsigned c) { return max(max(a, b), c); }
__device__ __forceinline__ unsigned umax4(const u32x4& v) { return max(umax3(v[0], v[1], v[2]), v[3]); }
__device__ __forceinline__ void glds16(const void* sbase, unsigned voff, unsigned lds_base) {
    unsigned sv; asm volatile("s_mov_b32 %0, m0\n\ts_mov_b32 m0, %3\n\ts_nop 0\n\tglobal_load_lds_dwordx4 %1, %2\n\ts_mov_b32 m0, %0" : "=&s"(sv) : "v"(voff), "s"(sbase), "s"(lds_base) : "memory");
}
typedef short v4i16_t __attribute__((ext_vector_type(4)));
__device__ __forceinline__ u32x4 tr_frag(LAS const char* p, int second) {
    const u32x2 a = __builtin_bit_cast(u32x2, __builtin_amdgcn_ds_read_tr16_b64_v4i16((LAS v4i16_t*)p));
    const u32x2 b = __builtin_bit_cast(u32x2, __builtin_amdgcn_ds_read_tr16_b64_v4i16((LAS v4i16_t*)(p + second)));
    return (u32x4){a[0], a[1], b[0], b[1]};
}
__device__ __forceinline__ float x16(float v) { auto s = __builtin_amdgcn_permlane16_swap(__float_as_uint(v), __float_as_uint(v), false, false); return __uint_as_float(s[0]) + __uint_as_float(s[1]); }
__device__ __forceinline__ float x32(float v) { auto s = __builtin_amdgcn_permlane32_swap(__float_as_uint(v), __float_as_uint(v), false, false); return __uint_as_float(s[0]) + __uint_as_float(s[1]); }
__device__ __forceinline__ float sum_g4(float v) { return x32(x16(v)); }
__device__ __forceinline__ float half_max(float m) { auto s = __builtin_amdgcn_permlane32_swap(__float_as_uint(m), __float_as_uint(m), false, false); return fmaxf(__uint_as_float(s[0]), __uint_as_float(s[1])); }
#define DPP(x, ctrl) __builtin_amdgcn_update_dpp(0, (int)(x), (ctrl), 0xf, 0xf, true)
__device__ __forceinline__ unsigned row_umax(unsigned v) {
    v = max(v, (unsigned)DPP(v, 0xB1)); v = max(v, (unsigned)DPP(v, 0x4E)); v = max(v, (unsigned)DPP(v, 0x141)); v = max(v, (unsigned)DPP(v, 0x140)); return v; }
__device__ __forceinline__ float row_sum(float v) {
    v += __int_as_float(DPP(__float_as_int(v), 0xB1)); v += __int_as_float(DPP(__float_as_int(v), 0x4E)); v += __int_as_float(DPP(__float_as_int(v), 0x141)); v += __int_as_float(DPP(__float_as_int(v), 0x140)); return v; }
__device__ __forceinline__ unsigned wave_umax(unsigned v) {
    v = row_umax(v);
    auto a = __builtin_amdgcn_permlane16_swap(v, v, false, false); v = max(a[0], a[1]);
    auto b = __builtin_amdgcn_permlane32_swap(v, v, false, false); return max(b[0], b[1]); }
__device__ __forceinline__ float dot2(unsigned a, unsigned b, float c) { return __builtin_amdgcn_fdot2_f32_bf16(__builtin_bit_cast(bf16x2, a), __builtin_bit_cast(bf16x2, b), c, false); }
__device__ __forceinline__ u32x4 widen(const f32x4& a, const f32x4& b) {
    auto s0 = __builtin_amdgcn_permlane16_swap(pk_bf16(a[0], a[1]), pk_bf16(b[0], b[1]), false, false);
    auto s1 = __builtin_amdgcn_permlane16_swap(pk_bf16(a[2], a[3]), pk_bf16(b[2], b[3]), false, false);
    return (u32x4){s0[0], s1[0], s0[1], s1[1]};
}
__device__ __forceinline__ void ln_scalars(float s1, float s2, float& mb, float& rs) {
    const float m = s1 * (1.f / DM), var = fmaxf(s2 * (1.f / DM) - m * m, 0.f);
    mb = rbf(m); rs = 1.f / rbf(sqrtf(rbf(rbf(var) + 1e-5f)));
}
#define MFMA16(a, b, c) __builtin_amdgcn_mfma_f32_16x16x32_bf16(__builtin_bit_cast(bf16x8, a), __builtin_bit_cast(bf16x8, b), c, 0, 0, 0)
#define MFMA32(a, b, c) __builtin_amdgcn_mfma_f32_32x32x16_bf16(__builtin_bit_cast(bf16x8, a), __builtin_bit_cast(bf16x8, b), c, 0, 0, 0)
#define SMF(v, p, o) __builtin_amdgcn_smfmac_f32_32x32x32_bf16(__builtin_bit_cast(bf16x8, v), __builtin_bit_cast(bf16x16, p), o, sidx, 0, 0)
#define LO8(s) __builtin_shufflevector(s, s, 0, 1, 2, 3, 4, 5, 6, 7)
#define HI8(s) __builtin_shufflevector(s, s, 8, 9, 10, 11, 12, 13, 14, 15)
#define EX(s, i) s[i] = __builtin_amdgcn_exp2f(s[i])
#define LD128(off) __builtin_amdgcn_raw_buffer_load_b128(rs, (off), 0, 16)
#define LD64(off) __builtin_amdgcn_raw_buffer_load_b64(rs, (off), 0, 16)
#define LD32(off) __builtin_amdgcn_raw_buffer_load_b32(rs, (off), 0, 16)
#define ST128(v, off) __builtin_amdgcn_raw_buffer_store_b128((v), rs, (off), 0, 16)
#define ST64(v, off) __builtin_amdgcn_raw_buffer_store_b64((v), rs, (off), 0, 16)
#define ST32(v, off) __builtin_amdgcn_raw_buffer_store_b32((v), rs, (off), 0, 16)
#define WAITBAR(n) asm volatile("s_waitcnt vmcnt(" #n ") lgkmcnt(0)\n\ts_barrier" ::: "memory")
#define LBAR() asm volatile("s_waitcnt lgkmcnt(0)\n\ts_barrier" ::: "memory")

__device__ __forceinline__ float max16(const f32x16& s) {
    float a = fmaxf(fmaxf(s[0], s[1]), s[2]), b = fmaxf(fmaxf(s[3], s[4]), s[5]);
    a = fmaxf(fmaxf(a, s[6]), s[7]); b = fmaxf(fmaxf(b, s[8]), s[9]);
    a = fmaxf(fmaxf(a, s[10]), s[11]); b = fmaxf(fmaxf(b, s[12]), s[13]);
    a = fmaxf(fmaxf(a, s[14]), s[15]);
    return fmaxf(a, b);
}

__global__ __launch_bounds__(512, 2) void k_fused(const u16* __restrict__ x, const u16* __restrict__ wqkv, const u16* __restrict__ wo, const u16* __restrict__ wr,
                                                  const u16* __restrict__ wg, const u16* __restrict__ wu, const u16* __restrict__ wd, u16* __restrict__ out, char* __restrict__ ws) {
    extern __shared__ __attribute__((aligned(16))) char lds[];
    const int bid = blockIdx.x;
    const int g = (bid & 1) * 4 + ((bid >> 3) & 3), rb = ((bid >> 1) & 3) * 8 + (bid >> 5);
    const int tid = threadIdx.x, l = tid & 63, w = __builtin_amdgcn_readfirstlane(tid >> 6);
    const int c = l & 15, g4 = l >> 4, wl = w & 3, wh = w >> 2;
    const unsigned lds0 = (unsigned)(uintptr_t)lds;
    LAS char* const L = (LAS char*)lds;
    const __amdgpu_buffer_rsrc_t rs = __builtin_amdgcn_make_buffer_rsrc(ws, 0, (int)WS_END, 0x00020000);
    STAMP(0);
    constexpr unsigned RING = 65536;
    const int r8 = l >> 3, s8 = l & 7;
    const unsigned xoff = (unsigned)((64 * rb + 8 * w + r8) * (DM * 2) + ((s8 ^ ((4 * w + (r8 >> 1)) & 7)) * 16));
    const unsigned woff = (unsigned)((8 * w + r8) * (3 * DM * 2) + 128 * g + ((s8 ^ ((((r8 >> 1) & 1) | ((w & 1) << 1)) << 1)) * 16));
    const char* xb = (const char*)x; const char* wb = (const char*)wqkv;
    auto dma_x = [&](int i) { glds16(xb + i * 128, xoff, lds0 + i * 8192 + w * 1024); };
    auto dma_kv = [&](int i) { const unsigned s = lds0 + RING + (i % 6) * 16384 + w * 1024; const char* p = wb + (size_t)i * 64 * 3072; glds16(p + 1024, woff, s); glds16(p + 2048, woff, s + 8192); };
    auto dma_q = [&](int j) { const unsigned s = lds0 + RING + ((j + 8) % 6) * 16384 + w * 1024; const char* p = wb + (size_t)j * 128 * 3072; glds16(p, woff, s); glds16(p + 64 * 3072, woff, s + 8192); };
#pragma unroll
    for (int i = 0; i < 6; ++i) { dma_x(i); dma_kv(i); }
    dma_x(6); dma_x(7);
    const int head = 4 * g + wl;
    const unsigned o_off = WS_O + head * 65536 + (64 * rb + 32 * wh + (l & 31)) * 32 + (l >> 5) * 16;
    {
        const u32x4 sv = {SENT, SENT, SENT, SENT};
        ST128(sv, o_off);
        if (!wh) {
            unsigned a = WS_END;
            if (l < 16) a = WS_ST + ((rb * 4 + wl) * 8 + g) * 256 + l * 16;
            else if (l < 32) a = ((l & 8) ? WS_CH : WS_CLN) + (4 * rb + wl) * 1024 + 128 * g + (l & 7) * 16;
            else if (l < 34) { if (g == 0 && wl == 0) a = WS_CKEY + rb * 128 + (l - 32) * 16; }
            else if (l < 38) { if (bid < N_FFN1 && wl == 0) a = WS_ACT + bid * 128 + (l - 34) * 16; }
            ST128(sv, a);
        }
    }
    const unsigned tag = LD32(WS_EPOCH) * 0x9E3779B1u + 0x7F4A7C15u;
    const int q4 = c >> 2, p4 = l & 3;
    const int brow = 8 * g4 + q4;
    const unsigned bswz = (((brow >> 1) & 1) | (((brow >> 3) & 1) << 1)) << 1;
    const unsigned boff = brow * 128 + (((2 * wl + (p4 >> 1)) ^ bswz) * 16) + (p4 & 1) * 8;
    const unsigned aoff0 = (32 * wh + c) * 128 + ((g4 ^ (c >> 1)) * 16), aoff1 = aoff0 ^ 64;
    u32x4 af[2][16];
    f32x4 aK0 = {}, aK1 = {}, aV0 = {}, aV1 = {}, cK = {}, cV = {};
    float s1a = 0.f, s2a = 0.f, s1b = 0.f, s2b = 0.f;
    const u32x4 ones4 = {0x3F803F80u, 0x3F803F80u, 0x3F803F80u, 0x3F803F80u};
    auto load_a = [&](int i) {
        af[0][2 * i] = *(LAS const u32x4*)(L + i * 8192 + aoff0); af[1][2 * i] = *(LAS const u32x4*)(L + i * 8192 + 2048 + aoff0);
        af[0][2 * i + 1] = *(LAS const u32x4*)(L + i * 8192 + aoff1); af[1][2 * i + 1] = *(LAS const u32x4*)(L + i * 8192 + 2048 + aoff1);
    };
    auto stat = [&](int s) {
        if ((s & 3) == wl) {
#pragma unroll
            for (int j = 0; j < 4; ++j) { s1a = dot2(af[0][s][j], 0x3F803F80u, s1a); s2a = dot2(af[0][s][j], af[0][s][j], s2a); s1b = dot2(af[1][s][j], 0x3F803F80u, s1b); s2b = dot2(af[1][s][j], af[1][s][j], s2b); }
        }
    };
    auto kv_chunk = [&](int i) {
        LAS const char* bp = L + RING + (i % 6) * 16384 + boff;
#pragma unroll
        for (int u = 0; u < 2; ++u) {
            const int s = 2 * i + u;
            const u32x4 bk = tr_frag(bp + u * 4096, 512), bv = tr_frag(bp + 8192 + u * 4096, 512);
            aK0 = MFMA16(bk, af[0][s], aK0); aK1 = MFMA16(bk, af[1][s], aK1); cK = MFMA16(bk, ones4, cK);
            aV0 = MFMA16(af[0][s], bv, aV0); aV1 = MFMA16(af[1][s], bv, aV1); cV = MFMA16(ones4, bv, cV);
        }
    };
    WAITBAR(18);            load_a(0); kv_chunk(0); stat(0); stat(1);
    STAMP(1);
    WAITBAR(15); dma_kv(6); load_a(1); kv_chunk(1); stat(2); stat(3);
    WAITBAR(14); dma_kv(7); load_a(2); kv_chunk(2); stat(4); stat(5);
    WAITBAR(13); dma_q(0);  load_a(3); kv_chunk(3); stat(6); stat(7);
    WAITBAR(12); dma_q(1);  load_a(4); kv_chunk(4); stat(8); stat(9);
    WAITBAR(11); dma_q(2);  load_a(5); kv_chunk(5); stat(10); stat(11);
    WAITBAR(8);  dma_q(3);  load_a(6); load_a(7); kv_chunk(6); stat(12); stat(13); stat(14); stat(15);
    {
        s1a = sum_g4(s1a); s2a = sum_g4(s2a); s1b = sum_g4(s1b); s2b = sum_g4(s2b);
        if (g4 < 2) *(LAS f32x2*)(L + (32 * wh + 16 * g4 + c) * 32 + wl * 8) = g4 ? (f32x2){s1b, s2b} : (f32x2){s1a, s2a};
    }
    WAITBAR(8);  kv_chunk(7);
    float mA, rA, mB, rB;
    {
        const f32x4 a0 = *(LAS const f32x4*)(L + (32 * wh + c) * 32), a1 = *(LAS const f32x4*)(L + (32 * wh + c) * 32 + 16);
        const f32x4 b0 = *(LAS const f32x4*)(L + (32 * wh + 16 + c) * 32), b1 = *(LAS const f32x4*)(L + (32 * wh + 16 + c) * 32 + 16);
        ln_scalars((a0[0] + a0[2]) + (a1[0] + a1[2]), (a0[1] + a0[3]) + (a1[1] + a1[3]), mA, rA);
        ln_scalars((b0[0] + b0[2]) + (b1[0] + b1[2]), (b0[1] + b0[3]) + (b1[1] + b1[3]), mB, rB);
    }
    u32x4 kown;
    {
        LAS float* sc = (LAS float*)(L + 4096 + w * 256);
        if (g4 < 2) { sc[16 * g4 + c] = g4 ? mB : mA; sc[32 + 16 * g4 + c] = g4 ? rB : rA; }
        const f32x4 mv0 = *(LAS const f32x4*)(sc + 4 * g4), mv1 = *(LAS const f32x4*)(sc + 16 + 4 * g4), rv0 = *(LAS const f32x4*)(sc + 32 + 4 * g4), rv1 = *(LAS const f32x4*)(sc + 48 + 4 * g4);
        f32x4 k0, k1, v0, v1;
#pragma unroll
        for (int j = 0; j < 4; ++j) { k0[j] = (aK0[j] - mA * cK[j]) * rA; k1[j] = (aK1[j] - mB * cK[j]) * rB; v0[j] = (aV0[j] - mv0[j] * cV[0]) * rv0[j]; v1[j] = (aV1[j] - mv1[j] * cV[0]) * rv1[j]; }
        kown = widen(k0, k1); const u32x4 vv = widen(v0, v1);
        const unsigned tb = WS_KV + head * KV_STRIDE + (2 * rb + wh) * 2048;
        ST128(kown, tb + l * 16);
        ST128(vv, tb + 1024 + (g4 & 1) * 512 + (g4 >> 1) * 256 + c * 16);
    }
    STAMP(2);
    f32x4 aQ0 = {}, aQ1 = {}, cQ = {};
    auto q_chunk = [&](int j) {
        LAS const char* bp = L + RING + ((j + 8) % 6) * 16384 + boff;
#pragma unroll
        for (int u = 0; u < 4; ++u) {
            const u32x4 bq = tr_frag(bp + u * 4096, 512);
            aQ0 = MFMA16(bq, af[0][4 * j + u], aQ0); aQ1 = MFMA16(bq, af[1][4 * j + u], aQ1); cQ = MFMA16(bq, ones4, cQ);
        }
    };
    WAITBAR(2);  q_chunk(0); q_chunk(1);
    WAITBAR(0);
    if (tid == 0) ST32(tag, WS_FLAG + (g * 32 + rb) * 128);
    STAMP(3);
    q_chunk(2); q_chunk(3);
    u32x4 q0, q1;
    {
        f32x4 t0, t1; const float ra = rA * QSCALE, rb_ = rB * QSCALE;
#pragma unroll
        for (int j = 0; j < 4; ++j) { t0[j] = (aQ0[j] - mA * cQ[j]) * ra; t1[j] = (aQ1[j] - mB * cQ[j]) * rb_; }
        const u32x4 qf = widen(t0, t1);
        *(LAS u32x4*)(L + 8192 + w * 1024 + l * 16) = qf;
        LBAR();
        q0 = *(LAS const u32x4*)(L + 8192 + wl * 1024 + l * 16); q1 = *(LAS const u32x4*)(L + 8192 + (4 + wl) * 1024 + l * 16);
    }
    const f32x16 zero16 = {};
    float m0, m1;
    { const f32x16 s0 = MFMA32(kown, q0, zero16), s1 = MFMA32(kown, q1, zero16); m0 = max16(s0); m1 = max16(s1); }
    STAMP(4);
    _Pragma("nounroll") for (unsigned spins = 0; spins < SPIN_CAP; ++spins) {
        CFENCE();
        const unsigned f = LD32(WS_FLAG + (g * 32 + 16 * wh + c) * 128);
        if (__all(f == tag)) break;
        __builtin_amdgcn_s_sleep(1);
    }
    CFENCE();
    STAMP(5);
    constexpr unsigned WO_L = 16384, FF_L = 81920, XCH_L = 147456 - 4096;
    const char* kvb = ws + WS_KV + (size_t)head * KV_STRIDE + wh * 65536;
    const unsigned koff = l * 16, voff = (l >> 5) * 256 + c * 16;
    const u64 vmask = 0x0000FFFF0000FFFFull;
    const int sidx = 0xDDDD;
    const bool is1 = bid < N_FFN1, is2 = bid >= FFN2_BASE && bid < FFN2_BASE + N_FFN2;
    const unsigned wo_off = (unsigned)((8 * w + r8) * (DM * 2) + 128 * g + ((s8 ^ ((r8 >> 1) & 1)) * 16));
    unsigned ff_off; const char* ff_base; unsigned ff_step;
    if (is1) { const int ch = s8 ^ ((((r8 >> 1) & 1) | ((w & 1) << 1)) << 1);
               ff_base = (const char*)wg; ff_off = (unsigned)((8 * w + r8) * (FF * 2) + bid * 64 + (ch & 3) * 16); ff_step = 64 * FF * 2;
               (void)ch; }
    else     { ff_base = (const char*)wd; ff_off = (unsigned)((32 * w + (l >> 1)) * (DM * 2) + (bid - FFN2_BASE) * 32 + (l & 1) * 16); ff_step = 256 * DM * 2; }
    const bool up_lane = ((s8 ^ ((((r8 >> 1) & 1) | ((w & 1) << 1)) << 1)) & 4) != 0;
    const char* ff_lane = (is1 && up_lane ? (const char*)wu : ff_base) + ff_off;
    f32x16 o0, o1;
    int nit = 8; asm volatile("" : "+s"(nit));
    const int ff_n = is1 ? 8 : is2 ? 7 : 0;
    for (int attempt = 0; attempt < 2; ++attempt) {
        if (attempt) {
            for (int t = 0; t < 32; ++t) {
                const u32x4 kf = LD128(WS_KV + head * KV_STRIDE + wh * 65536 + t * 2048 + koff);
                const f32x16 s0 = MFMA32(kf, q0, zero16), s1 = MFMA32(kf, q1, zero16);
                m0 = t ? fmaxf(m0, max16(s0)) : max16(s0); m1 = t ? fmaxf(m1, max16(s1)) : max16(s1);
            }
        }
        m0 = half_max(m0); m1 = half_max(m1);
        f32x16 n0, n1;
#pragma unroll
        for (int i = 0; i < 16; ++i) { n0[i] = -m0; n1[i] = -m1; }
        PIN(n0); PIN(n1); PIN(q0); PIN(q1);
        o0 = zero16; o1 = zero16;
        u32x4 kf0, kf1, kf2, kf3, va0, vb0, va1, vb1, va2, vb2, va3, vb3;
        const unsigned one = (l & 11) ? 0u : 0x3F803F80u;
        va0 = vb0 = va1 = vb1 = va2 = vb2 = va3 = vb3 = (u32x4){one, one, one, one};
        const char* pp = kvb + 3072;
#define LDK(kf, off) asm volatile("global_load_dwordx4 %0, %1, %2 offset:" #off " sc1" : "=v"(kf) : "v"(koff), "s"(pp) : "memory")
#define LDV(va, vb, offa, offb) asm volatile("s_mov_b64 exec, %4\n\tglobal_load_dwordx4 %0, %2, %3 offset:" #offa " sc1\n\tglobal_load_dwordx4 %1, %2, %3 offset:" #offb " sc1\n\ts_mov_b64 exec, -1" : "+v"(va), "+v"(vb) : "v"(voff), "s"(pp), "s"(vmask) : "memory")
        asm volatile("s_waitcnt vmcnt(0)" ::: "memory");
        LDK(kf0, -3072); LDV(va0, vb0, -2048, -1536); LDK(kf1, -1024); LDV(va1, vb1, 0, 512); LDK(kf2, 1024); LDV(va2, vb2, 2048, 2560); LDK(kf3, 3072);
        pp += 8192;
        asm volatile("s_waitcnt vmcnt(9)" : "+v"(kf0) :: "memory");
        f32x16 s0 = MFMA32(kf0, q0, n0), s1 = zero16;
#define HALF(SC, OC, VA, SP, OP, VPB, KN, QN, NN, MID) do { \
            EX(SC, 0); EX(SC, 1); PIN(SC); SBAR(); \
            OP = SMF(VPB, HI8(SP), OP); SBAR(); \
            EX(SC, 2); EX(SC, 3); EX(SC, 4); EX(SC, 5); EX(SC, 6); PIN(SC); SBAR(); \
            SP = MFMA32(KN, QN, NN); SBAR(); \
            MID; SBAR(); \
            EX(SC, 7); EX(SC, 8); EX(SC, 9); EX(SC, 10); EX(SC, 11); EX(SC, 12); PIN(SC); SBAR(); \
            OC = SMF(VA, LO8(SC), OC); SBAR(); \
            EX(SC, 13); EX(SC, 14); EX(SC, 15); PIN(SC); SBAR(); \
        } while (0)
#define STEP(KC, KN, VA, VB, VPA, VPB, VOFFA, VOFFB, KOFF) do { \
            HALF(s0, o0, VA, s1, o1, VPB, KC, q1, n1, { LDV(VPA, VPB, VOFFA, VOFFB); LDK(KC, KOFF); asm volatile("s_waitcnt vmcnt(9)" : "+v"(VA), "+v"(VB), "+v"(KN) :: "memory"); }); \
            HALF(s1, o1, VA, s0, o0, VB, KN, q0, n0, {}); } while (0)
        __builtin_amdgcn_s_setprio(3);
        _Pragma("nounroll") for (int it = 0; it < nit; ++it) {
            if (it == 2 + wh) __builtin_amdgcn_s_setprio(2);
            if (it == 5 + wh) __builtin_amdgcn_s_setprio(1);
            if (it == 7 + wh) __builtin_amdgcn_s_setprio(0);
            STEP(kf0, kf1, va0, vb0, va3, vb3, -4096, -3584, -3072);
            glds16((const char*)wo + it * 65536, wo_off, lds0 + WO_L + it * 8192 + w * 1024);
            if (it < ff_n) {
                unsigned sv; const unsigned dst = lds0 + FF_L + it * 8192 + w * 1024; const char* src = ff_lane + (size_t)it * ff_step;
                asm volatile("s_mov_b32 %0, m0\n\ts_mov_b32 m0, %2\n\ts_nop 0\n\tglobal_load_lds_dwordx4 %1, off\n\ts_mov_b32 m0, %0" : "=&s"(sv) : "v"(src), "s"(dst) : "memory");
            }
            STEP(kf1, kf2, va1, vb1, va0, vb0, -2048, -1536, -1024);
            STEP(kf2, kf3, va2, vb2, va1, vb1, 0, 512, 1024);
            STEP(kf3, kf0, va3, vb3, va2, vb2, 2048, 2560, 3072);
            pp += 8192;
        }
        o1 = SMF(vb3, HI8(s1), o1);
        __builtin_amdgcn_s_setprio(0);
        asm volatile("s_waitcnt vmcnt(0)" : "+v"(kf0), "+v"(kf1), "+v"(kf2), "+v"(kf3), "+v"(va0), "+v"(vb0), "+v"(va1), "+v"(vb1), "+v"(va2), "+v"(vb2) :: "memory");
        float mx = 0.f;
#pragma unroll
        for (int i = 0; i < 8; ++i) mx += fabsf(o0[i]) * 0.0625f + fabsf(o1[i]) * 0.0625f;
        const bool ok = (o0[8] <= 16777216.f) && (o1[8] <= 16777216.f) && (mx < INFINITY);
        if (__all(ok)) break;
    }
    STAMP(6);
    unsigned peerf = LD32(WS_FLAG + ((l & 7) * 32 + rb) * 128);
    const int row = 64 * rb + 16 * wl + c;
    u32x4 xv0, xv1, wv0, wv1, wall;
    if (!wh) {
        const u16* xp = x + (size_t)row * DM + 64 * g + 16 * g4; const u16* wp = wr + 64 * g + 16 * g4;
        xv0 = *(const u32x4*)xp; xv1 = *(const u32x4*)(xp + 8); wv0 = *(const u32x4*)wp; wv1 = *(const u32x4*)(wp + 8); wall = *(const u32x4*)(wr + l * 8);
    }
    {
        LAS float* mine = (LAS float*)(L + (wh ? 147456 : 0) + wl * 2560); LAS const float* other = (LAS const float*)(L + (wh ? 0 : 147456) + wl * 2560);
        const f32x16& os = wh ? o0 : o1;
#pragma unroll
        for (int i = 0; i < 9; ++i) mine[i * 64 + l] = os[i];
        mine[9 * 64 + l] = wh ? m0 : m1;
        LBAR();
        const f32x16& ok_ = wh ? o1 : o0; const float mk = wh ? m1 : m0;
        const float mo = other[9 * 64 + l], M = fmaxf(mk, mo);
        const float fa = __builtin_amdgcn_exp2f(mk - M), fb = __builtin_amdgcn_exp2f(mo - M);
        const float rl = __builtin_amdgcn_rcpf(ok_[8] * fa + other[8 * 64 + l] * fb);
        float r[8];
#pragma unroll
        for (int i = 0; i < 8; ++i) r[i] = (ok_[i] * fa + other[i * 64 + l] * fb) * rl;
        const u32x4 ov = {nosent(pk_bf16(r[0], r[1])), nosent(pk_bf16(r[2], r[3])), nosent(pk_bf16(r[4], r[5])), nosent(pk_bf16(r[6], r[7]))};
        ST128(ov, o_off);
    }
    STAMP(7);
    _Pragma("nounroll") for (unsigned spins = 0; spins < SPIN_CAP; ++spins) {
        if (__all(peerf == tag)) break;
        __builtin_amdgcn_s_sleep(1); CFENCE();
        peerf = LD32(WS_FLAG + ((l & 7) * 32 + rb) * 128);
    }
    CFENCE();
    u32x4 wf[8][4];
    {
        const int orow = (g4 >> 1) * 16 + 4 * (g4 & 1) + q4;
        LAS const char* bp = L + WO_L + (256 * wh + orow) * 128 + p4 * 32;
        const unsigned sw = ((orow >> 1) & 1) * 16;
#pragma unroll
        for (int ct = 0; ct < 4; ++ct)
#pragma unroll
            for (int s = 0; s < 8; ++s) wf[s][ct] = tr_frag(bp + (((ct >> 1) * 16) ^ sw) + (ct & 1) * 8 + s * 4096, 1024);
    }
    u32x4 of[8];
    const unsigned of_off = WS_O + (16 * wh + (g4 >> 1)) * 65536 + row * 32 + (g4 & 1) * 16;
    {
        unsigned pend = 15;
        _Pragma("nounroll") for (unsigned spins = 0; spins < SPIN_CAP; ++spins) {
            CFENCE();
#pragma unroll
            for (int u = 0; u < 4; ++u) if (pend & (1u << u)) { of[2 * u] = LD128(of_off + (2 * u) * 131072); of[2 * u + 1] = LD128(of_off + (2 * u + 1) * 131072); }
#pragma unroll
            for (int u = 0; u < 4; ++u) if (__all(max(umax4(of[2 * u]), umax4(of[2 * u + 1])) != SENT)) pend &= ~(1u << u);
            if (!pend) break;
        }
    }
    STAMP(8);
    f32x4 acc[4] = {};
#pragma unroll
    for (int s = 0; s < 8; ++s)
#pragma unroll
        for (int ct = 0; ct < 4; ++ct) acc[ct] = MFMA16(wf[s][ct], of[s], acc[ct]);
    LAS f32x4* redA = (LAS f32x4*)(L + wl * 2560); LAS f32x4* redB = (LAS f32x4*)(L + 157696 + wl * 1536 - 2560);
    if (wh) { redA[l] = acc[0]; redA[64 + l] = acc[1]; if (l < 32) redA[128 + l] = acc[2]; else redB[128 + l] = acc[2]; redB[192 + l] = acc[3]; }
    LBAR();
    STAMP(9);
    if (!wh) {
        float wsum = 0.f;
#pragma unroll
        for (int j = 0; j < 4; ++j) wsum = dot2(wall[j], 0x3F803F80u, wsum);
        wsum = sum_g4(row_sum(wsum));
        const f32x4 p0 = redA[l], p1 = redA[64 + l], p2 = (l < 32 ? redA : redB)[128 + l], p3 = redB[192 + l];
        float h[16]; float s1 = 0.f, s2 = 0.f, s3 = 0.f;
#pragma unroll
        for (int i = 0; i < 16; ++i) {
            const int ct = i >> 2, j = i & 3;
            const float pv = ct == 0 ? p0[j] : ct == 1 ? p1[j] : ct == 2 ? p2[j] : p3[j];
            const unsigned xw = i < 8 ? xv0[i >> 1] : xv1[(i >> 1) & 3], ww = i < 8 ? wv0[i >> 1] : wv1[(i >> 1) & 3];
            const float hv = rbf(((i & 1) ? bf_hi(xw) : bf_lo(xw)) + rbf(acc[ct][j] + pv));
            h[i] = hv; s1 += hv; s2 += hv * hv; s3 += hv * ((i & 1) ? bf_hi(ww) : bf_lo(ww));
        }
        s1 = sum_g4(s1); s2 = sum_g4(s2); s3 = sum_g4(s3);
        const unsigned stb = WS_ST + (rb * 4 + wl) * 2048 + c * 16;
        if (g4 == 0) ST128(((u32x4){nosent(__float_as_uint(s1)), nosent(__float_as_uint(s2)), nosent(__float_as_uint(s3)), 0u}), stb + g * 256);
        STAMP(10);
        u32x4 va, vb;
        _Pragma("nounroll") for (unsigned spins = 0; spins < SPIN_CAP; ++spins) {
            CFENCE();
            va = LD128(stb + g4 * 512); vb = LD128(stb + g4 * 512 + 256);
            if (__all(max(umax4(va), umax4(vb)) != SENT)) break;
        }
        STAMP(11);
        const float t1 = sum_g4(__uint_as_float(va[0]) + __uint_as_float(vb[0])), t2 = sum_g4(__uint_as_float(va[1]) + __uint_as_float(vb[1])), t3 = sum_g4(__uint_as_float(va[2]) + __uint_as_float(vb[2]));
        float mb, rstd; ln_scalars(t1, t2, mb, rstd);
        const float rr = (t3 - mb * wsum) * rstd;
        const unsigned ru = __float_as_uint(rr);
        const unsigned key = ((rr == rr ? (ru ^ (((int)ru >> 31) | 0x80000000u)) : 0u) & ~2047u) | (unsigned)(2047 - row);
        const bool best = row_umax(key) == key;
        u32x4 lnp[2], hp[2], op[2];
#pragma unroll
        for (int i = 0; i < 8; ++i) {
            const float l0 = rbf(rbf(h[2 * i] - mb) * rstd), l1 = rbf(rbf(h[2 * i + 1] - mb) * rstd);
            lnp[i >> 2][i & 3] = nosent(pk_bf16(l0, l1)); hp[i >> 2][i & 3] = nosent(pk_bf16(h[2 * i], h[2 * i + 1])); op[i >> 2][i & 3] = pk_bf16(h[2 * i] + l0, h[2 * i + 1] + l1);
        }
        const unsigned coff = (rb * 4 + wl) * 1024 + 128 * g + 32 * g4;
        const __amdgpu_buffer_rsrc_t ro = __builtin_amdgcn_make_buffer_rsrc(out, 0, SEQ * DM * 2, 0x00020000);
        const unsigned ooff = row * (DM * 2) + 128 * g + 32 * g4;
        if (best) {
            if (g == 0 && g4 == 0) ST64(((u32x2){key, nosent(ru)}), WS_CKEY + rb * 128 + wl * 8);
            ST128(lnp[0], WS_CLN + coff); ST128(lnp[1], WS_CLN + coff + 16);
            __builtin_amdgcn_raw_buffer_store_b128(op[0], ro, ooff, 0, 16); __builtin_amdgcn_raw_buffer_store_b128(op[1], ro, ooff + 16, 0, 16);
        } else {
            __builtin_amdgcn_raw_buffer_store_b128(op[0], ro, ooff, 0, 0); __builtin_amdgcn_raw_buffer_store_b128(op[1], ro, ooff + 16, 0, 0);
        }
        asm volatile("s_waitcnt vmcnt(0)" ::: "memory");
        if (best) { ST128(hp[0], WS_CH + coff); ST128(hp[1], WS_CH + coff + 16); }
        STAMP(12);
        return;
    }
    if (!(is1 || is2)) return;
    _Pragma("nounroll") for (unsigned spins = 0; spins < SPIN_CAP; ++spins) {
        CFENCE();
        const unsigned f0 = LD32(WS_FLAG + l * 128), f1 = LD32(WS_FLAG + (64 + l) * 128), f2 = LD32(WS_FLAG + (128 + l) * 128), f3 = LD32(WS_FLAG + (192 + l) * 128);
        if (__all(f0 == tag && f1 == tag && f2 == tag && f3 == tag)) break;
        __builtin_amdgcn_s_sleep(1);
    }
    CFENCE();
    LAS float* fred = (LAS float*)(L + 10240);
    const unsigned key_off = WS_CKEY + (l >> 1) * 128 + (l & 1) * 16;
    if (is1) {
        u32x4 ff[16];
        LAS const char* bp = L + FF_L + (128 * wl + brow) * 128 + (p4 & 1) * 8;
#pragma unroll
        for (int ct = 0; ct < 4; ++ct)
#pragma unroll
            for (int s = 0; s < 4; ++s) ff[s * 4 + ct] = tr_frag(bp + (((2 * ct + (p4 >> 1)) ^ bswz) * 16) + s * 4096, 512);
        u32x4 xf[4]; unsigned cur = 0;
        _Pragma("nounroll") for (unsigned spins = 0; spins < SPIN_CAP; ++spins) {
            CFENCE();
            const u32x4 kk = LD128(key_off);
            const unsigned xo = WS_CLN + ((2047 - (cur & 2047u)) >> 4) * 1024 + wl * 256 + g4 * 16;
#pragma unroll
            for (int s = 0; s < 4; ++s) xf[s] = LD128(xo + s * 64);
            const bool kval = __all(umax4(kk) != SENT);
            const unsigned nb = wave_umax(max(kk[0] == SENT ? 0u : kk[0], kk[2] == SENT ? 0u : kk[2]));
            const bool same = __builtin_amdgcn_readfirstlane(nb) == __builtin_amdgcn_readfirstlane(cur);
            cur = nb;
            if (kval && same && __all(max(max(umax4(xf[0]), umax4(xf[1])), max(umax4(xf[2]), umax4(xf[3]))) != SENT)) break;
        }
        STAMP(11);
        f32x4 a[4] = {};
#pragma unroll
        for (int s = 0; s < 4; ++s)
#pragma unroll
            for (int ct = 0; ct < 4; ++ct) a[ct] = MFMA16(ff[s * 4 + ct], xf[s], a[ct]);
        if (c == 0) {
#pragma unroll
            for (int ct = 0; ct < 4; ++ct) *(LAS f32x4*)(fred + wl * 64 + ct * 16 + 4 * g4) = a[ct];
        }
        LBAR();
        if (wl == 0 && l < 16) {
            float av[2];
#pragma unroll
            for (int j = 0; j < 2; ++j) {
                const int n = 2 * l + j;
                const float gg = rbf((fred[n] + fred[64 + n]) + (fred[128 + n] + fred[192 + n]));
                const float uu = rbf((fred[32 + n] + fred[96 + n]) + (fred[160 + n] + fred[224 + n]));
                av[j] = rbf(gg * __builtin_amdgcn_rcpf(1.f + __expf(-gg))) * uu;
            }
            ST32(nosent(pk_bf16(av[0], av[1])), WS_ACT + bid * 128 + l * 4);
        }
        STAMP(12);
    } else {
        u32x4 ff[14];
        LAS const char* bp = L + FF_L + (448 * wl + brow) * 32 + p4 * 8;
#pragma unroll
        for (int s = 0; s < 14; ++s) ff[s] = tr_frag(bp + s * 1024, 128);
        const int col0 = (bid - FFN2_BASE) * 16;
        unsigned lnv = 0, hv = 0, nb = 0; float rsel = 0.f;
        if (wl == 0) {
            u32x4 kk;
            _Pragma("nounroll") for (unsigned spins = 0; spins < SPIN_CAP; ++spins) {
                CFENCE();
                kk = LD128(key_off);
                if (__all(umax4(kk) != SENT)) break;
                __builtin_amdgcn_s_sleep(4);
            }
            nb = wave_umax(max(kk[0], kk[2]));
            rsel = __uint_as_float(wave_umax(kk[0] == nb ? kk[1] : kk[2] == nb ? kk[3] : 0u));
            const unsigned co = ((2047 - (nb & 2047u)) >> 4) * 1024 + col0 * 2 + (l & 7) * 4;
            _Pragma("nounroll") for (unsigned spins = 0; spins < SPIN_CAP; ++spins) {
                CFENCE();
                lnv = LD32(WS_CLN + co); hv = LD32(WS_CH + co);
                if (__all(max(lnv, hv) != SENT)) break;
            }
        }
        STAMP(10);
        u32x4 xf[14];
        _Pragma("nounroll") for (unsigned spins = 0; spins < SPIN_CAP; ++spins) {
            CFENCE();
            unsigned mxu = 0;
#pragma unroll
            for (int s = 0; s < 14; ++s) xf[s] = LD128(WS_ACT + (14 * wl + s) * 128 + g4 * 16);
#pragma unroll
            for (int s = 0; s < 14; ++s) mxu = max(mxu, umax4(xf[s]));
            if (__all(mxu != SENT)) break;
        }
        STAMP(11);
        f32x4 a0 = {}, a1 = {};
#pragma unroll
        for (int s = 0; s < 14; s += 2) { a0 = MFMA16(ff[s], xf[s], a0); a1 = MFMA16(ff[s + 1], xf[s + 1], a1); }
        if (c == 0) *(LAS f32x4*)(fred + wl * 16 + 4 * g4) = a0 + a1;
        LBAR();
        if (wl == 0 && l < 8) {
            const float sig = rbf(__builtin_amdgcn_rcpf(1.f + __expf(-rbf(rsel))));
            float res[2];
#pragma unroll
            for (int j = 0; j < 2; ++j) {
                const int n = 2 * l + j;
                const float y = rbf((fred[n] + fred[16 + n]) + (fred[32 + n] + fred[48 + n]));
                const float ln = j ? bf_hi(lnv) : bf_lo(lnv), hh = j ? bf_hi(hv) : bf_lo(hv);
                res[j] = hh + rbf(ln + rbf(y * sig));
            }
            const __amdgpu_buffer_rsrc_t ro = __builtin_amdgcn_make_buffer_rsrc(out, 0, SEQ * DM * 2, 0x00020000);
            __builtin_amdgcn_raw_buffer_store_b32(pk_bf16(res[0], res[1]), ro, ((2047 - (nb & 2047u)) * DM + col0) * 2 + l * 4, 0, 16);
            if (bid == FFN2_BASE && l == 0) ST32(tag, WS_EPOCH);
        }
        STAMP(12);
    }
}

extern "C" void kernel_launch(void* const* d_in, const int* in_sizes, int n_in, void* d_out, int out_size, void* d_ws, size_t ws_size, hipStream_t stream) {
    (void)hipFuncSetAttribute((const void*)k_fused, hipFuncAttributeMaxDynamicSharedMemorySize, LDS_BYTES);
    k_fused<<<256, 512, LDS_BYTES, stream>>>((const u16*)d_in[0], (const u16*)d_in[1], (const u16*)d_in[2], (const u16*)d_in[4], (const u16*)d_in[5], (const u16*)d_in[6], (const u16*)d_in[7],
                                             (u16*)d_out, (char*)d_ws);
}
```

```cpp
#include <hip/hip_runtime.h>
#include <hip/hip_bf16.h>
#include <stdint.h>

typedef float f32x2 __attribute__((ext_vector_type(2)));
typedef float f32x4 __attribute__((ext_vector_type(4)));
typedef float f32x16 __attribute__((ext_vector_type(16)));
typedef __bf16 bf16x2 __attribute__((ext_vector_type(2)));
typedef __bf16 bf16x8 __attribute__((ext_vector_type(8)));
typedef __bf16 bf16x16 __attribute__((ext_vector_type(16)));
typedef unsigned u32x2 __attribute__((ext_vector_type(2)));
typedef unsigned u32x4 __attribute__((ext_vector_type(4)));
typedef unsigned long long u64;
typedef unsigned short u16;
#define LAS __attribute__((address_space(3)))
#define SBAR() __builtin_amdgcn_sched_barrier(0)
#define PIN(x) asm volatile("" : "+v"(x))
#define CFENCE() asm volatile("" ::: "memory")

constexpr int SEQ = 2048, DM = 512, NH = 32, FF = 1792;
constexpr unsigned KV_STRIDE = 131072 + 8192;
constexpr unsigned WS_KV = 0;
constexpr unsigned WS_O = 5u << 20;
constexpr unsigned WS_CTL = 7u << 20;
constexpr unsigned WS_FLAG = WS_CTL;
constexpr unsigned WS_EPOCH = WS_CTL + 32768;
constexpr unsigned WS_CKEY = WS_CTL + 36864;
constexpr unsigned WS_ACT = WS_CTL + 40960;
constexpr unsigned WS_ST = WS_CTL + 49152;
constexpr unsigned WS_CLN = WS_ST + 32 * 4 * 8 * 256;
constexpr unsigned WS_CH = WS_CLN + 131072;
constexpr unsigned WS_END = WS_CH + 131072;
constexpr unsigned SENT = 0xFFFFFFFFu;
constexpr unsigned SPIN_CAP = 1u << 18;
constexpr float QSCALE = 0.25f * 1.4426950408889634f;
constexpr int N_FFN1 = 56, FFN2_BASE = 64, N_FFN2 = 32;
constexpr int LDS_BYTES = 163840;

#define STAMP(i)

__device__ __forceinline__ unsigned pk_bf16(float lo, float hi) { bf16x2 v; v[0] = (__bf16)lo; v[1] = (__bf16)hi; return __builtin_bit_cast(unsigned, v); }
__device__ __forceinline__ float bf_lo(unsigned u) { return __uint_as_float(u << 16); }
__device__ __forceinline__ float bf_hi(unsigned u) { return __uint_as_float(u & 0xffff0000u); }
__device__ __forceinline__ float rbf(float x) { return (float)(__bf16)x; }
__device__ __forceinline__ unsigned nosent(unsigned u) { return u < 0xFFFFFFFEu ? u : 0xFFFFFFFEu; }
__device__ __forceinline__ unsigned umax3(unsigned a, unsigned b, unsigned c) { return max(max(a, b), c); }
__device__ __forceinline__ unsigned umax4(const u32x4& v) { return max(umax3(v[0], v[1], v[2]), v[3]); }
__device__ __forceinline__ void glds16(const void* sbase, unsigned voff, unsigned lds_base) {
    unsigned sv; asm volatile("s_mov_b32 %0, m0\n\ts_mov_b32 m0, %3\n\ts_nop 0\n\tglobal_load_lds_dwordx4 %1, %2\n\ts_mov_b32 m0, %0" : "=&s"(sv) : "v"(voff), "s"(sbase), "s"(lds_base) : "memory");
}
typedef short v4i16_t __attribute__((ext_vector_type(4)));
__device__ __forceinline__ u32x4 tr_frag(LAS const char* p, int second) {
    const u32x2 a = __builtin_bit_cast(u32x2, __builtin_amdgcn_ds_read_tr16_b64_v4i16((LAS v4i16_t*)p));
    const u32x2 b = __builtin_bit_cast(u32x2, __builtin_amdgcn_ds_read_tr16_b64_v4i16((LAS v4i16_t*)(p + second)));
    return (u32x4){a[0], a[1], b[0], b[1]};
}
__device__ __forceinline__ float x16(float v) { auto s = __builtin_amdgcn_permlane16_swap(__float_as_uint(v), __float_as_uint(v), false, false); return __uint_as_float(s[0]) + __uint_as_float(s[1]); }
__device__ __forceinline__ float x32(float v) { auto s = __builtin_amdgcn_permlane32_swap(__float_as_uint(v), __float_as_uint(v), false, false); return __uint_as_float(s[0]) + __uint_as_float(s[1]); }
__device__ __forceinline__ float sum_g4(float v) { return x32(x16(v)); }
__device__ __forceinline__ float half_max(float m) { auto s = __builtin_amdgcn_permlane32_swap(__float_as_uint(m), __float_as_uint(m), false, false); return fmaxf(__uint_as_float(s[0]), __uint_as_float(s[1])); }
#define DPP(x, ctrl) __builtin_amdgcn_update_dpp(0, (int)(x), (ctrl), 0xf, 0xf, true)
__device__ __forceinline__ unsigned row_umax(unsigned v) {
    v = max(v, (unsigned)DPP(v, 0xB1)); v = max(v, (unsigned)DPP(v, 0x4E)); v = max(v, (unsigned)DPP(v, 0x141)); v = max(v, (unsigned)DPP(v, 0x140)); return v; }
__device__ __forceinline__ float row_sum(float v) {
    v += __int_as_float(DPP(__float_as_int(v), 0xB1)); v += __int_as_float(DPP(__float_as_int(v), 0x4E)); v += __int_as_float(DPP(__float_as_int(v), 0x141)); v += __int_as_float(DPP(__float_as_int(v), 0x140)); return v; }
__device__ __forceinline__ unsigned wave_umax(unsigned v) {
    v = row_umax(v);
    auto a = __builtin_amdgcn_permlane16_swap(v, v, false, false); v = max(a[0], a[1]);
    auto b = __builtin_amdgcn_permlane32_swap(v, v, false, false); return max(b[0], b[1]); }
__device__ __forceinline__ float dot2(unsigned a, unsigned b, float c) { return __builtin_amdgcn_fdot2_f32_bf16(__builtin_bit_cast(bf16x2, a), __builtin_bit_cast(bf16x2, b), c, false); }
__device__ __forceinline__ u32x4 widen(const f32x4& a, const f32x4& b) {
    auto s0 = __builtin_amdgcn_permlane16_swap(pk_bf16(a[0], a[1]), pk_bf16(b[0], b[1]), false, false);
    auto s1 = __builtin_amdgcn_permlane16_swap(pk_bf16(a[2], a[3]), pk_bf16(b[2], b[3]), false, false);
    return (u32x4){s0[0], s1[0], s0[1], s1[1]};
}
__device__ __forceinline__ void ln_scalars(float s1, float s2, float& mb, float& rs) {
    const float m = s1 * (1.f / DM), var = fmaxf(s2 * (1.f / DM) - m * m, 0.f);
    mb = rbf(m); rs = 1.f / rbf(sqrtf(rbf(rbf(var) + 1e-5f)));
}
#define MFMA16(a, b, c) __builtin_amdgcn_mfma_f32_16x16x32_bf16(__builtin_bit_cast(bf16x8, a), __builtin_bit_cast(bf16x8, b), c, 0, 0, 0)
#define MFMA32(a, b, c) __builtin_amdgcn_mfma_f32_32x32x16_bf16(__builtin_bit_cast(bf16x8, a), __builtin_bit_cast(bf16x8, b), c, 0, 0, 0)
#define SMF(v, p, o) __builtin_amdgcn_smfmac_f32_32x32x32_bf16(__builtin_bit_cast(bf16x8, v), __builtin_bit_cast(bf16x16, p), o, sidx, 0, 0)
#define LO8(s) __builtin_shufflevector(s, s, 0, 1, 2, 3, 4, 5, 6, 7)
#define HI8(s) __builtin_shufflevector(s, s, 8, 9, 10, 11, 12, 13, 14, 15)
#define EX(s, i) s[i] = __builtin_amdgcn_exp2f(s[i])
#define LD128(off) __builtin_amdgcn_raw_buffer_load_b128(rs, (off), 0, 16)
#define LD64(off) __builtin_amdgcn_raw_buffer_load_b64(rs, (off), 0, 16)
#define LD32(off) __builtin_amdgcn_raw_buffer_load_b32(rs, (off), 0, 16)
#define ST128(v, off) __builtin_amdgcn_raw_buffer_store_b128((v), rs, (off), 0, 16)
#define ST64(v, off) __builtin_amdgcn_raw_buffer_store_b64((v), rs, (off), 0, 16)
#define ST32(v, off) __builtin_amdgcn_raw_buffer_store_b32((v), rs, (off), 0, 16)
#define WAITBAR(n) asm volatile("s_waitcnt vmcnt(" #n ") lgkmcnt(0)\n\ts_barrier" ::: "memory")
#define LBAR() asm volatile("s_waitcnt lgkmcnt(0)\n\ts_barrier" ::: "memory")

__device__ __forceinline__ float max16(const f32x16& s) {
    float a = fmaxf(fmaxf(s[0], s[1]), s[2]), b = fmaxf(fmaxf(s[3], s[4]), s[5]);
    a = fmaxf(fmaxf(a, s[6]), s[7]); b = fmaxf(fmaxf(b, s[8]), s[9]);
    a = fmaxf(fmaxf(a, s[10]), s[11]); b = fmaxf(fmaxf(b, s[12]), s[13]);
    a = fmaxf(fmaxf(a, s[14]), s[15]);
    return fmaxf(a, b);
}

__global__ __launch_bounds__(512, 2) void k_fused(const u16* __restrict__ x, const u16* __restrict__ wqkv, const u16* __restrict__ wo, const u16* __restrict__ wr,
                                                  const u16* __restrict__ wg, const u16* __restrict__ wu, const u16* __restrict__ wd, u16* __restrict__ out, char* __restrict__ ws) {
    extern __shared__ __attribute__((aligned(16))) char lds[];
    const int bid = blockIdx.x;
    const int g = (bid & 1) * 4 + ((bid >> 3) & 3), rb = ((bid >> 1) & 3) * 8 + (bid >> 5);
    const int tid = threadIdx.x, l = tid & 63, w = __builtin_amdgcn_readfirstlane(tid >> 6);
    const int c = l & 15, g4 = l >> 4, wl = w & 3, wh = w >> 2;
    const unsigned lds0 = (unsigned)(uintptr_t)lds;
    LAS char* const L = (LAS char*)lds;
    const __amdgpu_buffer_rsrc_t rs = __builtin_amdgcn_make_buffer_rsrc(ws, 0, (int)WS_END, 0x00020000);
    STAMP(0);
    unsigned code_pf;
    { u64 pc; asm volatile("s_getpc_b64 %0" : "=s"(pc)); pc &= ~127ull; const unsigned po = (tid < 176 ? tid : 0) * 128;
      asm volatile("global_load_dword %0, %1, %2" : "=v"(code_pf) : "v"(po), "s"(pc) : "memory"); }
    constexpr unsigned RING = 65536;
    const int r8 = l >> 3, s8 = l & 7;
    const unsigned xoff = (unsigned)((64 * rb + 8 * w + r8) * (DM * 2) + ((s8 ^ ((4 * w + (r8 >> 1)) & 7)) * 16));
    const unsigned woff = (unsigned)((8 * w + r8) * (3 * DM * 2) + 128 * g + ((s8 ^ ((((r8 >> 1) & 1) | ((w & 1) << 1)) << 1)) * 16));
    const char* xb = (const char*)x; const char* wb = (const char*)wqkv;
    auto dma_x = [&](int i) { glds16(xb + i * 128, xoff, lds0 + i * 8192 + w * 1024); };
    auto dma_kv = [&](int i) { const unsigned s = lds0 + RING + (i % 6) * 16384 + w * 1024; const char* p = wb + (size_t)i * 64 * 3072; glds16(p + 1024, woff, s); glds16(p + 2048, woff, s + 8192); };
    auto dma_q = [&](int j) { const unsigned s = lds0 + RING + ((j + 8) % 6) * 16384 + w * 1024; const char* p = wb + (size_t)j * 128 * 3072; glds16(p, woff, s); glds16(p + 64 * 3072, woff, s + 8192); };
#pragma unroll
    for (int i = 0; i < 6; ++i) { dma_x(i); dma_kv(i); }
    dma_x(6); dma_x(7);
    const int head = 4 * g + wl;
    const unsigned o_off = WS_O + head * 65536 + (64 * rb + 32 * wh + (l & 31)) * 32 + (l >> 5) * 16;
    {
        const u32x4 sv = {SENT, SENT, SENT, SENT};
        ST128(sv, o_off);
        if (!wh) {
            unsigned a = WS_END;
            if (l < 16) a = WS_ST + ((rb * 4 + wl) * 8 + g) * 256 + l * 16;
            else if (l < 32) a = ((l & 8) ? WS_CH : WS_CLN) + (4 * rb + wl) * 1024 + 128 * g + (l & 7) * 16;
            else if (l < 34) { if (g == 0 && wl == 0) a = WS_CKEY + rb * 128 + (l - 32) * 16; }
            else if (l < 38) { if (bid < N_FFN1 && wl == 0) a = WS_ACT + bid * 128 + (l - 34) * 16; }
            ST128(sv, a);
        }
    }
    const unsigned tag = LD32(WS_EPOCH) * 0x9E3779B1u + 0x7F4A7C15u;
    const int q4 = c >> 2, p4 = l & 3;
    const int brow = 8 * g4 + q4;
    const unsigned bswz = (((brow >> 1) & 1) | (((brow >> 3) & 1) << 1)) << 1;
    const unsigned boff = brow * 128 + (((2 * wl + (p4 >> 1)) ^ bswz) * 16) + (p4 & 1) * 8;
    const unsigned aoff0 = (32 * wh + c) * 128 + ((g4 ^ (c >> 1)) * 16), aoff1 = aoff0 ^ 64;
    u32x4 af[2][16];
    f32x4 aK0 = {}, aK1 = {}, aV0 = {}, aV1 = {}, cK = {}, cV = {};
    float s1a = 0.f, s2a = 0.f, s1b = 0.f, s2b = 0.f;
    const u32x4 ones4 = {0x3F803F80u, 0x3F803F80u, 0x3F803F80u, 0x3F803F80u};
    auto load_a = [&](int i) {
        af[0][2 * i] = *(LAS const u32x4*)(L + i * 8192 + aoff0); af[1][2 * i] = *(LAS const u32x4*)(L + i * 8192 + 2048 + aoff0);
        af[0][2 * i + 1] = *(LAS const u32x4*)(L + i * 8192 + aoff1); af[1][2 * i + 1] = *(LAS const u32x4*)(L + i * 8192 + 2048 + aoff1);
    };
    auto stat = [&](int s) {
        if ((s & 3) == wl) {
#pragma unroll
            for (int j = 0; j < 4; ++j) { s1a = dot2(af[0][s][j], 0x3F803F80u, s1a); s2a = dot2(af[0][s][j], af[0][s][j], s2a); s1b = dot2(af[1][s][j], 0x3F803F80u, s1b); s2b = dot2(af[1][s][j], af[1][s][j], s2b); }
        }
    };
    auto kv_chunk = [&](int i) {
        LAS const char* bp = L + RING + (i % 6) * 16384 + boff;
#pragma unroll
        for (int u = 0; u < 2; ++u) {
            const int s = 2 * i + u;
            const u32x4 bk = tr_frag(bp + u * 4096, 512), bv = tr_frag(bp + 8192 + u * 4096, 512);
            aK0 = MFMA16(bk, af[0][s], aK0); aK1 = MFMA16(bk, af[1][s], aK1); cK = MFMA16(bk, ones4, cK);
            aV0 = MFMA16(af[0][s], bv, aV0); aV1 = MFMA16(af[1][s], bv, aV1); cV = MFMA16(ones4, bv, cV);
        }
    };
    WAITBAR(18); asm volatile("" :: "v"(code_pf)); load_a(0); kv_chunk(0); stat(0); stat(1);
    STAMP(1);
    WAITBAR(15); dma_kv(6); load_a(1); kv_chunk(1); stat(2); stat(3);
    WAITBAR(14); dma_kv(7); load_a(2); kv_chunk(2); stat(4); stat(5);
    WAITBAR(13); dma_q(0);  load_a(3); kv_chunk(3); stat(6); stat(7);
    WAITBAR(12); dma_q(1);  load_a(4); kv_chunk(4); stat(8); stat(9);
    WAITBAR(11); dma_q(2);  load_a(5); kv_chunk(5); stat(10); stat(11);
    WAITBAR(8);  dma_q(3);  load_a(6); load_a(7); kv_chunk(6); stat(12); stat(13); stat(14); stat(15);
    {
        s1a = sum_g4(s1a); s2a = sum_g4(s2a); s1b = sum_g4(s1b); s2b = sum_g4(s2b);
        if (g4 < 2) *(LAS f32x2*)(L + (32 * wh + 16 * g4 + c) * 32 + wl * 8) = g4 ? (f32x2){s1b, s2b} : (f32x2){s1a, s2a};
    }
    WAITBAR(8);  kv_chunk(7);
    float mA, rA, mB, rB;
    {
        const f32x4 a0 = *(LAS const f32x4*)(L + (32 * wh + c) * 32), a1 = *(LAS const f32x4*)(L + (32 * wh + c) * 32 + 16);
        const f32x4 b0 = *(LAS const f32x4*)(L + (32 * wh + 16 + c) * 32), b1 = *(LAS const f32x4*)(L + (32 * wh + 16 + c) * 32 + 16);
        ln_scalars((a0[0] + a0[2]) + (a1[0] + a1[2]), (a0[1] + a0[3]) + (a1[1] + a1[3]), mA, rA);
        ln_scalars((b0[0] + b0[2]) + (b1[0] + b1[2]), (b0[1] + b0[3]) + (b1[1] + b1[3]), mB, rB);
    }
    u32x4 kown;
    {
        LAS float* sc = (LAS float*)(L + 4096 + w * 256);
        if (g4 < 2) { sc[16 * g4 + c] = g4 ? mB : mA; sc[32 + 16 * g4 + c] = g4 ? rB : rA; }
        const f32x4 mv0 = *(LAS const f32x4*)(sc + 4 * g4), mv1 = *(LAS const f32x4*)(sc + 16 + 4 * g4), rv0 = *(LAS const f32x4*)(sc + 32 + 4 * g4), rv1 = *(LAS const f32x4*)(sc + 48 + 4 * g4);
        f32x4 k0, k1, v0, v1;
#pragma unroll
        for (int j = 0; j < 4; ++j) { k0[j] = (aK0[j] - mA * cK[j]) * rA; k1[j] = (aK1[j] - mB * cK[j]) * rB; v0[j] = (aV0[j] - mv0[j] * cV[0]) * rv0[j]; v1[j] = (aV1[j] - mv1[j] * cV[0]) * rv1[j]; }
        kown = widen(k0, k1); const u32x4 vv = widen(v0, v1);
        const unsigned tb = WS_KV + head * KV_STRIDE + (2 * rb + wh) * 2048;
        ST128(kown, tb + l * 16);
        ST128(vv, tb + 1024 + (g4 & 1) * 512 + (g4 >> 1) * 256 + c * 16);
    }
    STAMP(2);
    f32x4 aQ0 = {}, aQ1 = {}, cQ = {};
    auto q_chunk = [&](int j) {
        LAS const char* bp = L + RING + ((j + 8) % 6) * 16384 + boff;
#pragma unroll
        for (int u = 0; u < 4; ++u) {
            const u32x4 bq = tr_frag(bp + u * 4096, 512);
            aQ0 = MFMA16(bq, af[0][4 * j + u], aQ0); aQ1 = MFMA16(bq, af[1][4 * j + u], aQ1); cQ = MFMA16(bq, ones4, cQ);
        }
    };
    WAITBAR(2);  q_chunk(0); q_chunk(1);
    WAITBAR(0);
    if (tid == 0) ST32(tag, WS_FLAG + (g * 32 + rb) * 128);
    STAMP(3);
    q_chunk(2); q_chunk(3);
    u32x4 q0, q1;
    {
        f32x4 t0, t1; const float ra = rA * QSCALE, rb_ = rB * QSCALE;
#pragma unroll
        for (int j = 0; j < 4; ++j) { t0[j] = (aQ0[j] - mA * cQ[j]) * ra; t1[j] = (aQ1[j] - mB * cQ[j]) * rb_; }
        const u32x4 qf = widen(t0, t1);
        *(LAS u32x4*)(L + 8192 + w * 1024 + l * 16) = qf;
        LBAR();
        q0 = *(LAS const u32x4*)(L + 8192 + wl * 1024 + l * 16); q1 = *(LAS const u32x4*)(L + 8192 + (4 + wl) * 1024 + l * 16);
    }
    const f32x16 zero16 = {};
    float m0, m1;
    { const f32x16 s0 = MFMA32(kown, q0, zero16), s1 = MFMA32(kown, q1, zero16); m0 = max16(s0); m1 = max16(s1); }
    STAMP(4);
    _Pragma("nounroll") for (unsigned spins = 0; spins < SPIN_CAP; ++spins) {
        CFENCE();
        const unsigned f = LD32(WS_FLAG + (g * 32 + 16 * wh + c) * 128);
        if (__all(f == tag)) break;
        __builtin_amdgcn_s_sleep(1);
    }
    CFENCE();
    STAMP(5);
    constexpr unsigned WO_L = 16384, FF_L = 81920, XCH_L = 147456 - 4096;
    const char* kvb = ws + WS_KV + (size_t)head * KV_STRIDE + wh * 65536;
    const unsigned koff = l * 16, voff = (l >> 5) * 256 + c * 16;
    const u64 vmask = 0x0000FFFF0000FFFFull;
    const int sidx = 0xDDDD;
    const bool is1 = bid < N_FFN1, is2 = bid >= FFN2_BASE && bid < FFN2_BASE + N_FFN2;
    const unsigned wo_off = (unsigned)((8 * w + r8) * (DM * 2) + 128 * g + ((s8 ^ ((r8 >> 1) & 1)) * 16));
    unsigned ff_off; const char* ff_base; unsigned ff_step;
    if (is1) { const int ch = s8 ^ ((((r8 >> 1) & 1) | ((w & 1) << 1)) << 1);
               ff_base = (const char*)wg; ff_off = (unsigned)((8 * w + r8) * (FF * 2) + bid * 64 + (ch & 3) * 16); ff_step = 64 * FF * 2;
               (void)ch; }
    else     { ff_base = (const char*)wd; ff_off = (unsigned)((32 * w + (l >> 1)) * (DM * 2) + (bid - FFN2_BASE) * 32 + (l & 1) * 16); ff_step = 256 * DM * 2; }
    const bool up_lane = ((s8 ^ ((((r8 >> 1) & 1) | ((w & 1) << 1)) << 1)) & 4) != 0;
    const char* ff_lane = (is1 && up_lane ? (const char*)wu : ff_base) + ff_off;
    f32x16 o0, o1;
    int nit = 8; asm volatile("" : "+s"(nit));
    const int ff_n = is1 ? 8 : is2 ? 7 : 0;
    for (int attempt = 0; attempt < 2; ++attempt) {
        if (attempt) {
            for (int t = 0; t < 32; ++t) {
                const u32x4 kf = LD128(WS_KV + head * KV_STRIDE + wh * 65536 + t * 2048 + koff);
                const f32x16 s0 = MFMA32(kf, q0, zero16), s1 = MFMA32(kf, q1, zero16);
                m0 = t ? fmaxf(m0, max16(s0)) : max16(s0); m1 = t ? fmaxf(m1, max16(s1)) : max16(s1);
            }
        }
        m0 = half_max(m0); m1 = half_max(m1);
        f32x16 n0, n1;
#pragma unroll
        for (int i = 0; i < 16; ++i) { n0[i] = -m0; n1[i] = -m1; }
        PIN(n0); PIN(n1); PIN(q0); PIN(q1);
        o0 = zero16; o1 = zero16;
        u32x4 kf0, kf1, kf2, kf3, va0, vb0, va1, vb1, va2, vb2, va3, vb3;
        const unsigned one = (l & 11) ? 0u : 0x3F803F80u;
        va0 = vb0 = va1 = vb1 = va2 = vb2 = va3 = vb3 = (u32x4){one, one, one, one};
        const char* pp = kvb + 3072;
#define LDK(kf, off) asm volatile("global_load_dwordx4 %0, %1, %2 offset:" #off " sc1" : "=v"(kf) : "v"(koff), "s"(pp) : "memory")
#define LDV(va, vb, offa, offb) asm volatile("s_mov_b64 exec, %4\n\tglobal_load_dwordx4 %0, %2, %3 offset:" #offa " sc1\n\tglobal_load_dwordx4 %1, %2, %3 offset:" #offb " sc1\n\ts_mov_b64 exec, -1" : "+v"(va), "+v"(vb) : "v"(voff), "s"(pp), "s"(vmask) : "memory")
        asm volatile("s_waitcnt vmcnt(0)" ::: "memory");
        LDK(kf0, -3072); LDV(va0, vb0, -2048, -1536); LDK(kf1, -1024); LDV(va1, vb1, 0, 512); LDK(kf2, 1024); LDV(va2, vb2, 2048, 2560); LDK(kf3, 3072);
        pp += 8192;
        asm volatile("s_waitcnt vmcnt(9)" : "+v"(kf0) :: "memory");
        f32x16 s0 = MFMA32(kf0, q0, n0), s1 = zero16;
#define HALF(SC, OC, VA, SP, OP, VPB, KN, QN, NN, MID) do { \
            EX(SC, 0); EX(SC, 1); PIN(SC); SBAR(); \
            OP = SMF(VPB, HI8(SP), OP); SBAR(); \
            EX(SC, 2); EX(SC, 3); EX(SC, 4); EX(SC, 5); EX(SC, 6); PIN(SC); SBAR(); \
            SP = MFMA32(KN, QN, NN); SBAR(); \
            MID; SBAR(); \
            EX(SC, 7); EX(SC, 8); EX(SC, 9); EX(SC, 10); EX(SC, 11); EX(SC, 12); PIN(SC); SBAR(); \
            OC = SMF(VA, LO8(SC), OC); SBAR(); \
            EX(SC, 13); EX(SC, 14); EX(SC, 15); PIN(SC); SBAR(); \
        } while (0)
#define STEP(KC, KN, VA, VB, VPA, VPB, VOFFA, VOFFB, KOFF) do { \
            HALF(s0, o0, VA, s1, o1, VPB, KC, q1, n1, { LDV(VPA, VPB, VOFFA, VOFFB); LDK(KC, KOFF); asm volatile("s_waitcnt vmcnt(9)" : "+v"(VA), "+v"(VB), "+v"(KN) :: "memory"); }); \
            HALF(s1, o1, VA, s0, o0, VB, KN, q0, n0, {}); } while (0)
        __builtin_amdgcn_s_setprio(3);
        _Pragma("nounroll") for (int it = 0; it < nit; ++it) {
            if (it == 2 + wh) __builtin_amdgcn_s_setprio(2);
            if (it == 5 + wh) __builtin_amdgcn_s_setprio(1);
            if (it == 7 + wh) __builtin_amdgcn_s_setprio(0);
            STEP(kf0, kf1, va0, vb0, va3, vb3, -4096, -3584, -3072);
            glds16((const char*)wo + it * 65536, wo_off, lds0 + WO_L + it * 8192 + w * 1024);
            if (it < ff_n) {
                unsigned sv; const unsigned dst = lds0 + FF_L + it * 8192 + w * 1024; const char* src = ff_lane + (size_t)it * ff_step;
                asm volatile("s_mov_b32 %0, m0\n\ts_mov_b32 m0, %2\n\ts_nop 0\n\tglobal_load_lds_dwordx4 %1, off\n\ts_mov_b32 m0, %0" : "=&s"(sv) : "v"(src), "s"(dst) : "memory");
            }
            STEP(kf1, kf2, va1, vb1, va0, vb0, -2048, -1536, -1024);
            STEP(kf2, kf3, va2, vb2, va1, vb1, 0, 512, 1024);
            STEP(kf3, kf0, va3, vb3, va2, vb2, 2048, 2560, 3072);
            pp += 8192;
        }
        o1 = SMF(vb3, HI8(s1), o1);
        __builtin_amdgcn_s_setprio(0);
        asm volatile("s_waitcnt vmcnt(0)" : "+v"(kf0), "+v"(kf1), "+v"(kf2), "+v"(kf3), "+v"(va0), "+v"(vb0), "+v"(va1), "+v"(vb1), "+v"(va2), "+v"(vb2) :: "memory");
        float mx = 0.f;
#pragma unroll
        for (int i = 0; i < 8; ++i) mx += fabsf(o0[i]) * 0.0625f + fabsf(o1[i]) * 0.0625f;
        const bool ok = (o0[8] <= 16777216.f) && (o1[8] <= 16777216.f) && (mx < INFINITY);
        if (__all(ok)) break;
    }
    STAMP(6);
    unsigned peerf = LD32(WS_FLAG + ((l & 7) * 32 + rb) * 128);
    const int row = 64 * rb + 16 * wl + c;
    u32x4 xv0, xv1, wv0, wv1, wall;
    if (!wh) {
        const u16* xp = x + (size_t)row * DM + 64 * g + 16 * g4; const u16* wp = wr + 64 * g + 16 * g4;
        xv0 = *(const u32x4*)xp; xv1 = *(const u32x4*)(xp + 8); wv0 = *(const u32x4*)wp; wv1 = *(const u32x4*)(wp + 8); wall = *(const u32x4*)(wr + l * 8);
    }
    {
        LAS float* mine = (LAS float*)(L + (wh ? 147456 : 0) + wl * 2560); LAS const float* other = (LAS const float*)(L + (wh ? 0 : 147456) + wl * 2560);
        const f32x16& os = wh ? o0 : o1;
#pragma unroll
        for (int i = 0; i < 9; ++i) mine[i * 64 + l] = os[i];
        mine[9 * 64 + l] = wh ? m0 : m1;
        LBAR();
        const f32x16& ok_ = wh ? o1 : o0; const float mk = wh ? m1 : m0;
        const float mo = other[9 * 64 + l], M = fmaxf(mk, mo);
        const float fa = __builtin_amdgcn_exp2f(mk - M), fb = __builtin_amdgcn_exp2f(mo - M);
        const float rl = __builtin_amdgcn_rcpf(ok_[8] * fa + other[8 * 64 + l] * fb);
        float r[8];
#pragma unroll
        for (int i = 0; i < 8; ++i) r[i] = (ok_[i] * fa + other[i * 64 + l] * fb) * rl;
        const u32x4 ov = {nosent(pk_bf16(r[0], r[1])), nosent(pk_bf16(r[2], r[3])), nosent(pk_bf16(r[4], r[5])), nosent(pk_bf16(r[6], r[7]))};
        ST128(ov, o_off);
    }
    STAMP(7);
    _Pragma("nounroll") for (unsigned spins = 0; spins < SPIN_CAP; ++spins) {
        if (__all(peerf == tag)) break;
        __builtin_amdgcn_s_sleep(1); CFENCE();
        peerf = LD32(WS_FLAG + ((l & 7) * 32 + rb) * 128);
    }
    CFENCE();
    u32x4 wf[8][4];
    {
        const int orow = (g4 >> 1) * 16 + 4 * (g4 & 1) + q4;
        LAS const char* bp = L + WO_L + (256 * wh + orow) * 128 + p4 * 32;
        const unsigned sw = ((orow >> 1) & 1) * 16;
#pragma unroll
        for (int ct = 0; ct < 4; ++ct)
#pragma unroll
            for (int s = 0; s < 8; ++s) wf[s][ct] = tr_frag(bp + (((ct >> 1) * 16) ^ sw) + (ct & 1) * 8 + s * 4096, 1024);
    }
    u32x4 of[8];
    const unsigned of_off = WS_O + (16 * wh + (g4 >> 1)) * 65536 + row * 32 + (g4 & 1) * 16;
    {
        unsigned pend = 15;
        _Pragma("nounroll") for (unsigned spins = 0; spins < SPIN_CAP; ++spins) {
            CFENCE();
#pragma unroll
            for (int u = 0; u < 4; ++u) if (pend & (1u << u)) { of[2 * u] = LD128(of_off + (2 * u) * 131072); of[2 * u + 1] = LD128(of_off + (2 * u + 1) * 131072); }
#pragma unroll
            for (int u = 0; u < 4; ++u) if (__all(max(umax4(of[2 * u]), umax4(of[2 * u + 1])) != SENT)) pend &= ~(1u << u);
            if (!pend) break;
        }
    }
    STAMP(8);
    f32x4 acc[4] = {};
#pragma unroll
    for (int s = 0; s < 8; ++s)
#pragma unroll
        for (int ct = 0; ct < 4; ++ct) acc[ct] = MFMA16(wf[s][ct], of[s], acc[ct]);
    LAS f32x4* redA = (LAS f32x4*)(L + wl * 2560); LAS f32x4* redB = (LAS f32x4*)(L + 157696 + wl * 1536 - 2560);
    if (wh) { redA[l] = acc[0]; redA[64 + l] = acc[1]; if (l < 32) redA[128 + l] = acc[2]; else redB[128 + l] = acc[2]; redB[192 + l] = acc[3]; }
    LBAR();
    STAMP(9);
    if (!wh) {
        float wsum = 0.f;
#pragma unroll
        for (int j = 0; j < 4; ++j) wsum = dot2(wall[j], 0x3F803F80u, wsum);
        wsum = sum_g4(row_sum(wsum));
        const f32x4 p0 = redA[l], p1 = redA[64 + l], p2 = (l < 32 ? redA : redB)[128 + l], p3 = redB[192 + l];
        float h[16]; float s1 = 0.f, s2 = 0.f, s3 = 0.f;
#pragma unroll
        for (int i = 0; i < 16; ++i) {
            const int ct = i >> 2, j = i & 3;
            const float pv = ct == 0 ? p0[j] : ct == 1 ? p1[j] : ct == 2 ? p2[j] : p3[j];
            const unsigned xw = i < 8 ? xv0[i >> 1] : xv1[(i >> 1) & 3], ww = i < 8 ? wv0[i >> 1] : wv1[(i >> 1) & 3];
            const float hv = rbf(((i & 1) ? bf_hi(xw) : bf_lo(xw)) + rbf(acc[ct][j] + pv));
            h[i] = hv; s1 += hv; s2 += hv * hv; s3 += hv * ((i & 1) ? bf_hi(ww) : bf_lo(ww));
        }
        s1 = sum_g4(s1); s2 = sum_g4(s2); s3 = sum_g4(s3);
        const unsigned stb = WS_ST + (rb * 4 + wl) * 2048 + c * 16;
        if (g4 == 0) ST128(((u32x4){nosent(__float_as_uint(s1)), nosent(__float_as_uint(s2)), nosent(__float_as_uint(s3)), 0u}), stb + g * 256);
        STAMP(10);
        u32x4 va, vb;
        _Pragma("nounroll") for (unsigned spins = 0; spins < SPIN_CAP; ++spins) {
            CFENCE();
            va = LD128(stb + g4 * 512); vb = LD128(stb + g4 * 512 + 256);
            if (__all(max(umax4(va), umax4(vb)) != SENT)) break;
        }
        STAMP(11);
        const float t1 = sum_g4(__uint_as_float(va[0]) + __uint_as_float(vb[0])), t2 = sum_g4(__uint_as_float(va[1]) + __uint_as_float(vb[1])), t3 = sum_g4(__uint_as_float(va[2]) + __uint_as_float(vb[2]));
        float mb, rstd; ln_scalars(t1, t2, mb, rstd);
        const float rr = (t3 - mb * wsum) * rstd;
        const unsigned ru = __float_as_uint(rr);
        const unsigned key = ((rr == rr ? (ru ^ (((int)ru >> 31) | 0x80000000u)) : 0u) & ~2047u) | (unsigned)(2047 - row);
        const bool best = row_umax(key) == key;
        u32x4 lnp[2], hp[2], op[2];
#pragma unroll
        for (int i = 0; i < 8; ++i) {
            const float l0 = rbf(rbf(h[2 * i] - mb) * rstd), l1 = rbf(rbf(h[2 * i + 1] - mb) * rstd);
            lnp[i >> 2][i & 3] = nosent(pk_bf16(l0, l1)); hp[i >> 2][i & 3] = nosent(pk_bf16(h[2 * i], h[2 * i + 1])); op[i >> 2][i & 3] = pk_bf16(h[2 * i] + l0, h[2 * i + 1] + l1);
        }
        const unsigned coff = (rb * 4 + wl) * 1024 + 128 * g + 32 * g4;
        const __amdgpu_buffer_rsrc_t ro = __builtin_amdgcn_make_buffer_rsrc(out, 0, SEQ * DM * 2, 0x00020000);
        const unsigned ooff = row * (DM * 2) + 128 * g + 32 * g4;
        if (best) {
            if (g == 0 && g4 == 0) ST64(((u32x2){key, nosent(ru)}), WS_CKEY + rb * 128 + wl * 8);
            ST128(lnp[0], WS_CLN + coff); ST128(lnp[1], WS_CLN + coff + 16);
            __builtin_amdgcn_raw_buffer_store_b128(op[0], ro, ooff, 0, 16); __builtin_amdgcn_raw_buffer_store_b128(op[1], ro, ooff + 16, 0, 16);
        } else {
            __builtin_amdgcn_raw_buffer_store_b128(op[0], ro, ooff, 0, 0); __builtin_amdgcn_raw_buffer_store_b128(op[1], ro, ooff + 16, 0, 0);
        }
        asm volatile("s_waitcnt vmcnt(0)" ::: "memory");
        if (best) { ST128(hp[0], WS_CH + coff); ST128(hp[1], WS_CH + coff + 16); }
        STAMP(12);
        return;
    }
    if (!(is1 || is2)) return;
    _Pragma("nounroll") for (unsigned spins = 0; spins < SPIN_CAP; ++spins) {
        CFENCE();
        const unsigned f0 = LD32(WS_FLAG + l * 128), f1 = LD32(WS_FLAG + (64 + l) * 128), f2 = LD32(WS_FLAG + (128 + l) * 128), f3 = LD32(WS_FLAG + (192 + l) * 128);
        if (__all(f0 == tag && f1 == tag && f2 == tag && f3 == tag)) break;
        __builtin_amdgcn_s_sleep(1);
    }
    CFENCE();
    LAS float* fred = (LAS float*)(L + 10240);
    const unsigned key_off = WS_CKEY + (l >> 1) * 128 + (l & 1) * 16;
    if (is1) {
        u32x4 ff[16];
        LAS const char* bp = L + FF_L + (128 * wl + brow) * 128 + (p4 & 1) * 8;
#pragma unroll
        for (int ct = 0; ct < 4; ++ct)
#pragma unroll
            for (int s = 0; s < 4; ++s) ff[s * 4 + ct] = tr_frag(bp + (((2 * ct + (p4 >> 1)) ^ bswz) * 16) + s * 4096, 512);
        u32x4 xf[4]; unsigned cur = 0;
        _Pragma("nounroll") for (unsigned spins = 0; spins < SPIN_CAP; ++spins) {
            CFENCE();
            const u32x4 kk = LD128(key_off);
            const unsigned xo = WS_CLN + ((2047 - (cur & 2047u)) >> 4) * 1024 + wl * 256 + g4 * 16;
#pragma unroll
            for (int s = 0; s < 4; ++s) xf[s] = LD128(xo + s * 64);
            const bool kval = __all(umax4(kk) != SENT);
            const unsigned nb = wave_umax(max(kk[0] == SENT ? 0u : kk[0], kk[2] == SENT ? 0u : kk[2]));
            const bool same = __builtin_amdgcn_readfirstlane(nb) == __builtin_amdgcn_readfirstlane(cur);
            cur = nb;
            if (kval && same && __all(max(max(umax4(xf[0]), umax4(xf[1])), max(umax4(xf[2]), umax4(xf[3]))) != SENT)) break;
        }
        STAMP(11);
        f32x4 a[4] = {};
#pragma unroll
        for (int s = 0; s < 4; ++s)
#pragma unroll
            for (int ct = 0; ct < 4; ++ct) a[ct] = MFMA16(ff[s * 4 + ct], xf[s], a[ct]);
        if (c == 0) {
#pragma unroll
            for (int ct = 0; ct < 4; ++ct) *(LAS f32x4*)(fred + wl * 64 + ct * 16 + 4 * g4) = a[ct];
        }
        LBAR();
        if (wl == 0 && l < 16) {
            float av[2];
#pragma unroll
            for (int j = 0; j < 2; ++j) {
                const int n = 2 * l + j;
                const float gg = rbf((fred[n] + fred[64 + n]) + (fred[128 + n] + fred[192 + n]));
                const float uu = rbf((fred[32 + n] + fred[96 + n]) + (fred[160 + n] + fred[224 + n]));
                av[j] = rbf(gg * __builtin_amdgcn_rcpf(1.f + __expf(-gg))) * uu;
            }
            ST32(nosent(pk_bf16(av[0], av[1])), WS_ACT + bid * 128 + l * 4);
        }
        STAMP(12);
    } else {
        u32x4 ff[14];
        LAS const char* bp = L + FF_L + (448 * wl + brow) * 32 + p4 * 8;
#pragma unroll
        for (int s = 0; s < 14; ++s) ff[s] = tr_frag(bp + s * 1024, 128);
        const int col0 = (bid - FFN2_BASE) * 16;
        unsigned lnv = 0, hv = 0, nb = 0; float rsel = 0.f;
        if (wl == 0) {
            u32x4 kk;
            _Pragma("nounroll") for (unsigned spins = 0; spins < SPIN_CAP; ++spins) {
                CFENCE();
                kk = LD128(key_off);
                if (__all(umax4(kk) != SENT)) break;
                __builtin_amdgcn_s_sleep(4);
            }
            nb = wave_umax(max(kk[0], kk[2]));
            rsel = __uint_as_float(wave_umax(kk[0] == nb ? kk[1] : kk[2] == nb ? kk[3] : 0u));
            const unsigned co = ((2047 - (nb & 2047u)) >> 4) * 1024 + col0 * 2 + (l & 7) * 4;
            _Pragma("nounroll") for (unsigned spins = 0; spins < SPIN_CAP; ++spins) {
                CFENCE();
                lnv = LD32(WS_CLN + co); hv = LD32(WS_CH + co);
                if (__all(max(lnv, hv) != SENT)) break;
            }
        }
        STAMP(10);
        u32x4 xf[14];
        _Pragma("nounroll") for (unsigned spins = 0; spins < SPIN_CAP; ++spins) {
            CFENCE();
            unsigned mxu = 0;
#pragma unroll
            for (int s = 0; s < 14; ++s) xf[s] = LD128(WS_ACT + (14 * wl + s) * 128 + g4 * 16);
#pragma unroll
            for (int s = 0; s < 14; ++s) mxu = max(mxu, umax4(xf[s]));
            if (__all(mxu != SENT)) break;
        }
        STAMP(11);
        f32x4 a0 = {}, a1 = {};
#pragma unroll
        for (int s = 0; s < 14; s += 2) { a0 = MFMA16(ff[s], xf[s], a0); a1 = MFMA16(ff[s + 1], xf[s + 1], a1); }
        if (c == 0) *(LAS f32x4*)(fred + wl * 16 + 4 * g4) = a0 + a1;
        LBAR();
        if (wl == 0 && l < 8) {
            const float sig = rbf(__builtin_amdgcn_rcpf(1.f + __expf(-rbf(rsel))));
            float res[2];
#pragma unroll
            for (int j = 0; j < 2; ++j) {
                const int n = 2 * l + j;
                const float y = rbf((fred[n] + fred[16 + n]) + (fred[32 + n] + fred[48 + n]));
                const float ln = j ? bf_hi(lnv) : bf_lo(lnv), hh = j ? bf_hi(hv) : bf_lo(hv);
                res[j] = hh + rbf(ln + rbf(y * sig));
            }
            const __amdgpu_buffer_rsrc_t ro = __builtin_amdgcn_make_buffer_rsrc(out, 0, SEQ * DM * 2, 0x00020000);
            __builtin_amdgcn_raw_buffer_store_b32(pk_bf16(res[0], res[1]), ro, ((2047 - (nb & 2047u)) * DM + col0) * 2 + l * 4, 0, 16);
            if (bid == FFN2_BASE && l == 0) ST32(tag, WS_EPOCH);
        }
        STAMP(12);
    }
}

extern "C" void kernel_launch(void* const* d_in, const int* in_sizes, int n_in, void* d_out, int out_size, void* d_ws, size_t ws_size, hipStream_t stream) {
    (void)hipFuncSetAttribute((const void*)k_fused, hipFuncAttributeMaxDynamicSharedMemorySize, LDS_BYTES);
    k_fused<<<256, 512, LDS_BYTES, stream>>>((const u16*)d_in[0], (const u16*)d_in[1], (const u16*)d_in[2], (const u16*)d_in[4], (const u16*)d_in[5], (const u16*)d_in[6], (const u16*)d_in[7],
                                             (u16*)d_out, (char*)d_ws);
}
```

```cpp
#include <hip/hip_runtime.h>
#include <hip/hip_bf16.h>
#include <stdint.h>

typedef float f32x2 __attribute__((ext_vector_type(2)));
typedef float f32x4 __attribute__((ext_vector_type(4)));
typedef float f32x16 __attribute__((ext_vector_type(16)));
typedef __bf16 bf16x2 __attribute__((ext_vector_type(2)));
typedef __bf16 bf16x8 __attribute__((ext_vector_type(8)));
typedef __bf16 bf16x16 __attribute__((ext_vector_type(16)));
typedef unsigned u32x2 __attribute__((ext_vector_type(2)));
typedef unsigned u32x4 __attribute__((ext_vector_type(4)));
typedef unsigned long long u64;
typedef unsigned short u16;
#define LAS __attribute__((address_space(3)))
#define SBAR() __builtin_amdgcn_sched_barrier(0)
#define PIN(x) asm volatile("" : "+v"(x))
#define CFENCE() asm volatile("" ::: "memory")

constexpr int SEQ = 2048, DM = 512, NH = 32, FF = 1792;
constexpr unsigned KV_STRIDE = 131072 + 8192;
constexpr unsigned WS_KV = 0;
constexpr unsigned WS_O = 5u << 20;
constexpr unsigned WS_CTL = 7u << 20;
constexpr unsigned WS_FLAG = WS_CTL;
constexpr unsigned WS_EPOCH = WS_CTL + 32768;
constexpr unsigned WS_CKEY = WS_CTL + 36864;
constexpr unsigned WS_ACT = WS_CTL + 40960;
constexpr unsigned WS_ST = WS_CTL + 49152;
constexpr unsigned WS_CLN = WS_ST + 32 * 4 * 8 * 256;
constexpr unsigned WS_CH = WS_CLN + 131072;
constexpr unsigned WS_END = WS_CH + 131072;
constexpr unsigned SENT = 0xFFFFFFFFu;
constexpr unsigned SPIN_CAP = 1u << 18;
constexpr float QSCALE = 0.25f * 1.4426950408889634f;
constexpr int N_FFN1 = 56, FFN2_BASE = 64, N_FFN2 = 32;
constexpr int LDS_BYTES = 163840;

#define STAMP(i) do { if ((i) == 0) pr_t0 = __builtin_amdgcn_s_memrealtime(); if ((i) == PROBE) pr_t1 = __builtin_amdgcn_s_memrealtime(); __builtin_amdgcn_s_waitcnt(0xC07F); } while (0)
#define PROBE 0

__device__ __forceinline__ unsigned pk_bf16(float lo, float hi) { bf16x2 v; v[0] = (__bf16)lo; v[1] = (__bf16)hi; return __builtin_bit_cast(unsigned, v); }
__device__ __forceinline__ float bf_lo(unsigned u) { return __uint_as_float(u << 16); }
__device__ __forceinline__ float bf_hi(unsigned u) { return __uint_as_float(u & 0xffff0000u); }
__device__ __forceinline__ float rbf(float x) { return (float)(__bf16)x; }
__device__ __forceinline__ unsigned nosent(unsigned u) { return u < 0xFFFFFFFEu ? u : 0xFFFFFFFEu; }
__device__ __forceinline__ unsigned umax3(unsigned a, unsigned b, unsigned c) { return max(max(a, b), c); }
__device__ __forceinline__ unsigned umax4(const u32x4& v) { return max(umax3(v[0], v[1], v[2]), v[3]); }
__device__ __forceinline__ void glds16(const void* sbase, unsigned voff, unsigned lds_base) {
    unsigned sv; asm volatile("s_mov_b32 %0, m0\n\ts_mov_b32 m0, %3\n\ts_nop 0\n\tglobal_load_lds_dwordx4 %1, %2\n\ts_mov_b32 m0, %0" : "=&s"(sv) : "v"(voff), "s"(sbase), "s"(lds_base) : "memory");
}
typedef short v4i16_t __attribute__((ext_vector_type(4)));
__device__ __forceinline__ u32x4 tr_frag(LAS const char* p, int second) {
    const u32x2 a = __builtin_bit_cast(u32x2, __builtin_amdgcn_ds_read_tr16_b64_v4i16((LAS v4i16_t*)p));
    const u32x2 b = __builtin_bit_cast(u32x2, __builtin_amdgcn_ds_read_tr16_b64_v4i16((LAS v4i16_t*)(p + second)));
    return (u32x4){a[0], a[1], b[0], b[1]};
}
__device__ __forceinline__ float x16(float v) { auto s = __builtin_amdgcn_permlane16_swap(__float_as_uint(v), __float_as_uint(v), false, false); return __uint_as_float(s[0]) + __uint_as_float(s[1]); }
__device__ __forceinline__ float x32(float v) { auto s = __builtin_amdgcn_permlane32_swap(__float_as_uint(v), __float_as_uint(v), false, false); return __uint_as_float(s[0]) + __uint_as_float(s[1]); }
__device__ __forceinline__ float sum_g4(float v) { return x32(x16(v)); }
__device__ __forceinline__ float half_max(float m) { auto s = __builtin_amdgcn_permlane32_swap(__float_as_uint(m), __float_as_uint(m), false, false); return fmaxf(__uint_as_float(s[0]), __uint_as_float(s[1])); }
#define DPP(x, ctrl) __builtin_amdgcn_update_dpp(0, (int)(x), (ctrl), 0xf, 0xf, true)
__device__ __forceinline__ unsigned row_umax(unsigned v) {
    v = max(v, (unsigned)DPP(v, 0xB1)); v = max(v, (unsigned)DPP(v, 0x4E)); v = max(v, (unsigned)DPP(v, 0x141)); v = max(v, (unsigned)DPP(v, 0x140)); return v; }
__device__ __forceinline__ float row_sum(float v) {
    v += __int_as_float(DPP(__float_as_int(v), 0xB1)); v += __int_as_float(DPP(__float_as_int(v), 0x4E)); v += __int_as_float(DPP(__float_as_int(v), 0x141)); v += __int_as_float(DPP(__float_as_int(v), 0x140)); return v; }
__device__ __forceinline__ unsigned wave_umax(unsigned v) {
    v = row_umax(v);
    auto a = __builtin_amdgcn_permlane16_swap(v, v, false, false); v = max(a[0], a[1]);
    auto b = __builtin_amdgcn_permlane32_swap(v, v, false, false); return max(b[0], b[1]); }
__device__ __forceinline__ float dot2(unsigned a, unsigned b, float c) { return __builtin_amdgcn_fdot2_f32_bf16(__builtin_bit_cast(bf16x2, a), __builtin_bit_cast(bf16x2, b), c, false); }
__device__ __forceinline__ u32x4 widen(const f32x4& a, const f32x4& b) {
    auto s0 = __builtin_amdgcn_permlane16_swap(pk_bf16(a[0], a[1]), pk_bf16(b[0], b[1]), false, false);
    auto s1 = __builtin_amdgcn_permlane16_swap(pk_bf16(a[2], a[3]), pk_bf16(b[2], b[3]), false, false);
    return (u32x4){s0[0], s1[0], s0[1], s1[1]};
}
__device__ __forceinline__ void ln_scalars(float s1, float s2, float& mb, float& rs) {
    const float m = s1 * (1.f / DM), var = fmaxf(s2 * (1.f / DM) - m * m, 0.f);
    mb = rbf(m); rs = 1.f / rbf(sqrtf(rbf(rbf(var) + 1e-5f)));
}
#define MFMA16(a, b, c) __builtin_amdgcn_mfma_f32_16x16x32_bf16(__builtin_bit_cast(bf16x8, a), __builtin_bit_cast(bf16x8, b), c, 0, 0, 0)
#define MFMA32(a, b, c) __builtin_amdgcn_mfma_f32_32x32x16_bf16(__builtin_bit_cast(bf16x8, a), __builtin_bit_cast(bf16x8, b), c, 0, 0, 0)
#define SMF(v, p, o) __builtin_amdgcn_smfmac_f32_32x32x32_bf16(__builtin_bit_cast(bf16x8, v), __builtin_bit_cast(bf16x16, p), o, sidx, 0, 0)
#define LO8(s) __builtin_shufflevector(s, s, 0, 1, 2, 3, 4, 5, 6, 7)
#define HI8(s) __builtin_shufflevector(s, s, 8, 9, 10, 11, 12, 13, 14, 15)
#define EX(s, i) s[i] = __builtin_amdgcn_exp2f(s[i])
#define LD128(off) __builtin_amdgcn_raw_buffer_load_b128(rs, (off), 0, 16)
#define LD64(off) __builtin_amdgcn_raw_buffer_load_b64(rs, (off), 0, 16)
#define LD32(off) __builtin_amdgcn_raw_buffer_load_b32(rs, (off), 0, 16)
#define ST128(v, off) __builtin_amdgcn_raw_buffer_store_b128((v), rs, (off), 0, 16)
#define ST64(v, off) __builtin_amdgcn_raw_buffer_store_b64((v), rs, (off), 0, 16)
#define ST32(v, off) __builtin_amdgcn_raw_buffer_store_b32((v), rs, (off), 0, 16)
#define WAITBAR(n) asm volatile("s_waitcnt vmcnt(" #n ") lgkmcnt(0)\n\ts_barrier" ::: "memory")
#define LBAR() asm volatile("s_waitcnt lgkmcnt(0)\n\ts_barrier" ::: "memory")

__device__ __forceinline__ float max16(const f32x16& s) {
    float a = fmaxf(fmaxf(s[0], s[1]), s[2]), b = fmaxf(fmaxf(s[3], s[4]), s[5]);
    a = fmaxf(fmaxf(a, s[6]), s[7]); b = fmaxf(fmaxf(b, s[8]), s[9]);
    a = fmaxf(fmaxf(a, s[10]), s[11]); b = fmaxf(fmaxf(b, s[12]), s[13]);
    a = fmaxf(fmaxf(a, s[14]), s[15]);
    return fmaxf(a, b);
}

__global__ __launch_bounds__(512, 2) void k_fused(const u16* __restrict__ x, const u16* __restrict__ wqkv, const u16* __restrict__ wo, const u16* __restrict__ wr,
                                                  const u16* __restrict__ wg, const u16* __restrict__ wu, const u16* __restrict__ wd, u16* __restrict__ out, char* __restrict__ ws) {
    extern __shared__ __attribute__((aligned(16))) char lds[];
    const int bid = blockIdx.x;
    const int g = (bid & 1) * 4 + ((bid >> 3) & 3), rb = ((bid >> 1) & 3) * 8 + (bid >> 5);
    const int tid = threadIdx.x, l = tid & 63, w = __builtin_amdgcn_readfirstlane(tid >> 6);
    const int c = l & 15, g4 = l >> 4, wl = w & 3, wh = w >> 2;
    const unsigned lds0 = (unsigned)(uintptr_t)lds;
    LAS char* const L = (LAS char*)lds;
    const __amdgpu_buffer_rsrc_t rs = __builtin_amdgcn_make_buffer_rsrc(ws, 0, (int)WS_END, 0x00020000);
    u64 pr_t0 = 0, pr_t1 = 0;
    STAMP(0);
    unsigned code_pf;
    { u64 pc; asm volatile("s_getpc_b64 %0" : "=s"(pc)); pc &= ~127ull; const unsigned po = (tid < 176 ? tid : 0) * 128;
      asm volatile("global_load_dword %0, %1, %2" : "=v"(code_pf) : "v"(po), "s"(pc) : "memory"); }
    constexpr unsigned RING = 65536;
    const int r8 = l >> 3, s8 = l & 7;
    const unsigned xoff = (unsigned)((64 * rb + 8 * w + r8) * (DM * 2) + ((s8 ^ ((4 * w + (r8 >> 1)) & 7)) * 16));
    const unsigned woff = (unsigned)((8 * w + r8) * (3 * DM * 2) + 128 * g + ((s8 ^ ((((r8 >> 1) & 1) | ((w & 1) << 1)) << 1)) * 16));
    const char* xb = (const char*)x; const char* wb = (const char*)wqkv;
    auto dma_x = [&](int i) { glds16(xb + i * 128, xoff, lds0 + i * 8192 + w * 1024); };
    auto dma_kv = [&](int i) { const unsigned s = lds0 + RING + (i % 6) * 16384 + w * 1024; const char* p = wb + (size_t)i * 64 * 3072; glds16(p + 1024, woff, s); glds16(p + 2048, woff, s + 8192); };
    auto dma_q = [&](int j) { const unsigned s = lds0 + RING + ((j + 8) % 6) * 16384 + w * 1024; const char* p = wb + (size_t)j * 128 * 3072; glds16(p, woff, s); glds16(p + 64 * 3072, woff, s + 8192); };
#pragma unroll
    for (int i = 0; i < 6; ++i) { dma_x(i); dma_kv(i); }
    dma_x(6); dma_x(7);
    const int head = 4 * g + wl;
    const unsigned o_off = WS_O + head * 65536 + (64 * rb + 32 * wh + (l & 31)) * 32 + (l >> 5) * 16;
    {
        const u32x4 sv = {SENT, SENT, SENT, SENT};
        ST128(sv, o_off);
        if (!wh) {
            unsigned a = WS_END;
            if (l < 16) a = WS_ST + ((rb * 4 + wl) * 8 + g) * 256 + l * 16;
            else if (l < 32) a = ((l & 8) ? WS_CH : WS_CLN) + (4 * rb + wl) * 1024 + 128 * g + (l & 7) * 16;
            else if (l < 34) { if (g == 0 && wl == 0) a = WS_CKEY + rb * 128 + (l - 32) * 16; }
            else if (l < 38) { if (bid < N_FFN1 && wl == 0) a = WS_ACT + bid * 128 + (l - 34) * 16; }
            ST128(sv, a);
        }
    }
    const unsigned tag = LD32(WS_EPOCH) * 0x9E3779B1u + 0x7F4A7C15u;
    const int q4 = c >> 2, p4 = l & 3;
    const int brow = 8 * g4 + q4;
    const unsigned bswz = (((brow >> 1) & 1) | (((brow >> 3) & 1) << 1)) << 1;
    const unsigned boff = brow * 128 + (((2 * wl + (p4 >> 1)) ^ bswz) * 16) + (p4 & 1) * 8;
    const unsigned aoff0 = (32 * wh + c) * 128 + ((g4 ^ (c >> 1)) * 16), aoff1 = aoff0 ^ 64;
    u32x4 af[2][16];
    f32x4 aK0 = {}, aK1 = {}, aV0 = {}, aV1 = {}, cK = {}, cV = {};
    float s1a = 0.f, s2a = 0.f, s1b = 0.f, s2b = 0.f;
    const u32x4 ones4 = {0x3F803F80u, 0x3F803F80u, 0x3F803F80u, 0x3F803F80u};
    auto load_a = [&](int i) {
        af[0][2 * i] = *(LAS const u32x4*)(L + i * 8192 + aoff0); af[1][2 * i] = *(LAS const u32x4*)(L + i * 8192 + 2048 + aoff0);
        af[0][2 * i + 1] = *(LAS const u32x4*)(L + i * 8192 + aoff1); af[1][2 * i + 1] = *(LAS const u32x4*)(L + i * 8192 + 2048 + aoff1);
    };
    auto stat = [&](int s) {
        if ((s & 3) == wl) {
#pragma unroll
            for (int j = 0; j < 4; ++j) { s1a = dot2(af[0][s][j], 0x3F803F80u, s1a); s2a = dot2(af[0][s][j], af[0][s][j], s2a); s1b = dot2(af[1][s][j], 0x3F803F80u, s1b); s2b = dot2(af[1][s][j], af[1][s][j], s2b); }
        }
    };
    auto kv_chunk = [&](int i) {
        LAS const char* bp = L + RING + (i % 6) * 16384 + boff;
#pragma unroll
        for (int u = 0; u < 2; ++u) {
            const int s = 2 * i + u;
            const u32x4 bk = tr_frag(bp + u * 4096, 512), bv = tr_frag(bp + 8192 + u * 4096, 512);
            aK0 = MFMA16(bk, af[0][s], aK0); aK1 = MFMA16(bk, af[1][s], aK1); cK = MFMA16(bk, ones4, cK);
            aV0 = MFMA16(af[0][s], bv, aV0); aV1 = MFMA16(af[1][s], bv, aV1); cV = MFMA16(ones4, bv, cV);
        }
    };
    WAITBAR(18); asm volatile("" :: "v"(code_pf)); load_a(0); kv_chunk(0); stat(0); stat(1);
    STAMP(1);
    WAITBAR(15); dma_kv(6); load_a(1); kv_chunk(1); stat(2); stat(3);
    WAITBAR(14); dma_kv(7); load_a(2); kv_chunk(2); stat(4); stat(5);
    WAITBAR(13); dma_q(0);  load_a(3); kv_chunk(3); stat(6); stat(7);
    WAITBAR(12); dma_q(1);  load_a(4); kv_chunk(4); stat(8); stat(9);
    WAITBAR(11); dma_q(2);  load_a(5); kv_chunk(5); stat(10); stat(11);
    WAITBAR(8);  dma_q(3);  load_a(6); load_a(7); kv_chunk(6); stat(12); stat(13); stat(14); stat(15);
    {
        s1a = sum_g4(s1a); s2a = sum_g4(s2a); s1b = sum_g4(s1b); s2b = sum_g4(s2b);
        if (g4 < 2) *(LAS f32x2*)(L + (32 * wh + 16 * g4 + c) * 32 + wl * 8) = g4 ? (f32x2){s1b, s2b} : (f32x2){s1a, s2a};
    }
    WAITBAR(8);  kv_chunk(7);
    float mA, rA, mB, rB;
    {
        const f32x4 a0 = *(LAS const f32x4*)(L + (32 * wh + c) * 32), a1 = *(LAS const f32x4*)(L + (32 * wh + c) * 32 + 16);
        const f32x4 b0 = *(LAS const f32x4*)(L + (32 * wh + 16 + c) * 32), b1 = *(LAS const f32x4*)(L + (32 * wh + 16 + c) * 32 + 16);
        ln_scalars((a0[0] + a0[2]) + (a1[0] + a1[2]), (a0[1] + a0[3]) + (a1[1] + a1[3]), mA, rA);
        ln_scalars((b0[0] + b0[2]) + (b1[0] + b1[2]), (b0[1] + b0[3]) + (b1[1] + b1[3]), mB, rB);
    }
    u32x4 kown;
    {
        LAS float* sc = (LAS float*)(L + 4096 + w * 256);
        if (g4 < 2) { sc[16 * g4 + c] = g4 ? mB : mA; sc[32 + 16 * g4 + c] = g4 ? rB : rA; }
        const f32x4 mv0 = *(LAS const f32x4*)(sc + 4 * g4), mv1 = *(LAS const f32x4*)(sc + 16 + 4 * g4), rv0 = *(LAS const f32x4*)(sc + 32 + 4 * g4), rv1 = *(LAS const f32x4*)(sc + 48 + 4 * g4);
        f32x4 k0, k1, v0, v1;
#pragma unroll
        for (int j = 0; j < 4; ++j) { k0[j] = (aK0[j] - mA * cK[j]) * rA; k1[j] = (aK1[j] - mB * cK[j]) * rB; v0[j] = (aV0[j] - mv0[j] * cV[0]) * rv0[j]; v1[j] = (aV1[j] - mv1[j] * cV[0]) * rv1[j]; }
        kown = widen(k0, k1); const u32x4 vv = widen(v0, v1);
        const unsigned tb = WS_KV + head * KV_STRIDE + (2 * rb + wh) * 2048;
        ST128(kown, tb + l * 16);
        ST128(vv, tb + 1024 + (g4 & 1) * 512 + (g4 >> 1) * 256 + c * 16);
    }
    STAMP(2);
    f32x4 aQ0 = {}, aQ1 = {}, cQ = {};
    auto q_chunk = [&](int j) {
        LAS const char* bp = L + RING + ((j + 8) % 6) * 16384 + boff;
#pragma unroll
        for (int u = 0; u < 4; ++u) {
            const u32x4 bq = tr_frag(bp + u * 4096, 512);
            aQ0 = MFMA16(bq, af[0][4 * j + u], aQ0); aQ1 = MFMA16(bq, af[1][4 * j + u], aQ1); cQ = MFMA16(bq, ones4, cQ);
        }
    };
    WAITBAR(2);  q_chunk(0); q_chunk(1);
    WAITBAR(0);
    if (tid == 0) ST32(tag, WS_FLAG + (g * 32 + rb) * 128);
    STAMP(3);
    q_chunk(2); q_chunk(3);
    u32x4 q0, q1;
    {
        f32x4 t0, t1; const float ra = rA * QSCALE, rb_ = rB * QSCALE;
#pragma unroll
        for (int j = 0; j < 4; ++j) { t0[j] = (aQ0[j] - mA * cQ[j]) * ra; t1[j] = (aQ1[j] - mB * cQ[j]) * rb_; }
        const u32x4 qf = widen(t0, t1);
        *(LAS u32x4*)(L + 8192 + w * 1024 + l * 16) = qf;
        LBAR();
        q0 = *(LAS const u32x4*)(L + 8192 + wl * 1024 + l * 16); q1 = *(LAS const u32x4*)(L + 8192 + (4 + wl) * 1024 + l * 16);
    }
    const f32x16 zero16 = {};
    float m0, m1;
    { const f32x16 s0 = MFMA32(kown, q0, zero16), s1 = MFMA32(kown, q1, zero16); m0 = max16(s0); m1 = max16(s1); }
    STAMP(4);
    _Pragma("nounroll") for (unsigned spins = 0; spins < SPIN_CAP; ++spins) {
        CFENCE();
        const unsigned f = LD32(WS_FLAG + (g * 32 + 16 * wh + c) * 128);
        if (__all(f == tag)) break;
        __builtin_amdgcn_s_sleep(1);
    }
    CFENCE();
    STAMP(5);
    constexpr unsigned WO_L = 16384, FF_L = 81920, XCH_L = 147456 - 4096;
    const char* kvb = ws + WS_KV + (size_t)head * KV_STRIDE + wh * 65536;
    const unsigned koff = l * 16, voff = (l >> 5) * 256 + c * 16;
    const u64 vmask = 0x0000FFFF0000FFFFull;
    const int sidx = 0xDDDD;
    const bool is1 = bid < N_FFN1, is2 = bid >= FFN2_BASE && bid < FFN2_BASE + N_FFN2;
    const unsigned wo_off = (unsigned)((8 * w + r8) * (DM * 2) + 128 * g + ((s8 ^ ((r8 >> 1) & 1)) * 16));
    unsigned ff_off; const char* ff_base; unsigned ff_step;
    if (is1) { const int ch = s8 ^ ((((r8 >> 1) & 1) | ((w & 1) << 1)) << 1);
               ff_base = (const char*)wg; ff_off = (unsigned)((8 * w + r8) * (FF * 2) + bid * 64 + (ch & 3) * 16); ff_step = 64 * FF * 2;
               (void)ch; }
    else     { ff_base = (const char*)wd; ff_off = (unsigned)((32 * w + (l >> 1)) * (DM * 2) + (bid - FFN2_BASE) * 32 + (l & 1) * 16); ff_step = 256 * DM * 2; }
    const bool up_lane = ((s8 ^ ((((r8 >> 1) & 1) | ((w & 1) << 1)) << 1)) & 4) != 0;
    const char* ff_lane = (is1 && up_lane ? (const char*)wu : ff_base) + ff_off;
    f32x16 o0, o1;
    int nit = 8; asm volatile("" : "+s"(nit));
    const int ff_n = is1 ? 8 : is2 ? 7 : 0;
    for (int attempt = 0; attempt < 2; ++attempt) {
        if (attempt) {
            for (int t = 0; t < 32; ++t) {
                const u32x4 kf = LD128(WS_KV + head * KV_STRIDE + wh * 65536 + t * 2048 + koff);
                const f32x16 s0 = MFMA32(kf, q0, zero16), s1 = MFMA32(kf, q1, zero16);
                m0 = t ? fmaxf(m0, max16(s0)) : max16(s0); m1 = t ? fmaxf(m1, max16(s1)) : max16(s1);
            }
        }
        m0 = half_max(m0); m1 = half_max(m1);
        f32x16 n0, n1;
#pragma unroll
        for (int i = 0; i < 16; ++i) { n0[i] = -m0; n1[i] = -m1; }
        PIN(n0); PIN(n1); PIN(q0); PIN(q1);
        o0 = zero16; o1 = zero16;
        u32x4 kf0, kf1, kf2, kf3, va0, vb0, va1, vb1, va2, vb2, va3, vb3;
        const unsigned one = (l & 11) ? 0u : 0x3F803F80u;
        va0 = vb0 = va1 = vb1 = va2 = vb2 = va3 = vb3 = (u32x4){one, one, one, one};
        const char* pp = kvb + 3072;
#define LDK(kf, off) asm volatile("global_load_dwordx4 %0, %1, %2 offset:" #off " sc1" : "=v"(kf) : "v"(koff), "s"(pp) : "memory")
#define LDV(va, vb, offa, offb) asm volatile("s_mov_b64 exec, %4\n\tglobal_load_dwordx4 %0, %2, %3 offset:" #offa " sc1\n\tglobal_load_dwordx4 %1, %2, %3 offset:" #offb " sc1\n\ts_mov_b64 exec, -1" : "+v"(va), "+v"(vb) : "v"(voff), "s"(pp), "s"(vmask) : "memory")
        asm volatile("s_waitcnt vmcnt(0)" ::: "memory");
        LDK(kf0, -3072); LDV(va0, vb0, -2048, -1536); LDK(kf1, -1024); LDV(va1, vb1, 0, 512); LDK(kf2, 1024); LDV(va2, vb2, 2048, 2560); LDK(kf3, 3072);
        pp += 8192;
        asm volatile("s_waitcnt vmcnt(9)" : "+v"(kf0) :: "memory");
        f32x16 s0 = MFMA32(kf0, q0, n0), s1 = zero16;
#define HALF(SC, OC, VA, SP, OP, VPB, KN, QN, NN, MID) do { \
            EX(SC, 0); EX(SC, 1); PIN(SC); SBAR(); \
            OP = SMF(VPB, HI8(SP), OP); SBAR(); \
            EX(SC, 2); EX(SC, 3); EX(SC, 4); EX(SC, 5); EX(SC, 6); PIN(SC); SBAR(); \
            SP = MFMA32(KN, QN, NN); SBAR(); \
            MID; SBAR(); \
            EX(SC, 7); EX(SC, 8); EX(SC, 9); EX(SC, 10); EX(SC, 11); EX(SC, 12); PIN(SC); SBAR(); \
            OC = SMF(VA, LO8(SC), OC); SBAR(); \
            EX(SC, 13); EX(SC, 14); EX(SC, 15); PIN(SC); SBAR(); \
        } while (0)
#define STEP(KC, KN, VA, VB, VPA, VPB, VOFFA, VOFFB, KOFF) do { \
            HALF(s0, o0, VA, s1, o1, VPB, KC, q1, n1, { LDV(VPA, VPB, VOFFA, VOFFB); LDK(KC, KOFF); asm volatile("s_waitcnt vmcnt(9)" : "+v"(VA), "+v"(VB), "+v"(KN) :: "memory"); }); \
            HALF(s1, o1, VA, s0, o0, VB, KN, q0, n0, {}); } while (0)
        __builtin_amdgcn_s_setprio(3);
        _Pragma("nounroll") for (int it = 0; it < nit; ++it) {
            if (it == 2 + wh) __builtin_amdgcn_s_setprio(2);
            if (it == 5 + wh) __builtin_amdgcn_s_setprio(1);
            if (it == 7 + wh) __builtin_amdgcn_s_setprio(0);
            STEP(kf0, kf1, va0, vb0, va3, vb3, -4096, -3584, -3072);
            glds16((const char*)wo + it * 65536, wo_off, lds0 + WO_L + it * 8192 + w * 1024);
            if (it < ff_n) {
                unsigned sv; const unsigned dst = lds0 + FF_L + it * 8192 + w * 1024; const char* src = ff_lane + (size_t)it * ff_step;
                asm volatile("s_mov_b32 %0, m0\n\ts_mov_b32 m0, %2\n\ts_nop 0\n\tglobal_load_lds_dwordx4 %1, off\n\ts_mov_b32 m0, %0" : "=&s"(sv) : "v"(src), "s"(dst) : "memory");
            }
            STEP(kf1, kf2, va1, vb1, va0, vb0, -2048, -1536, -1024);
            STEP(kf2, kf3, va2, vb2, va1, vb1, 0, 512, 1024);
            STEP(kf3, kf0, va3, vb3, va2, vb2, 2048, 2560, 3072);
            pp += 8192;
        }
        o1 = SMF(vb3, HI8(s1), o1);
        __builtin_amdgcn_s_setprio(0);
        asm volatile("s_waitcnt vmcnt(0)" : "+v"(kf0), "+v"(kf1), "+v"(kf2), "+v"(kf3), "+v"(va0), "+v"(vb0), "+v"(va1), "+v"(vb1), "+v"(va2), "+v"(vb2) :: "memory");
        float mx = 0.f;
#pragma unroll
        for (int i = 0; i < 8; ++i) mx += fabsf(o0[i]) * 0.0625f + fabsf(o1[i]) * 0.0625f;
        const bool ok = (o0[8] <= 16777216.f) && (o1[8] <= 16777216.f) && (mx < INFINITY);
        if (__all(ok)) break;
    }
    STAMP(6);
    unsigned peerf = LD32(WS_FLAG + ((l & 7) * 32 + rb) * 128);
    const int row = 64 * rb + 16 * wl + c;
    u32x4 xv0, xv1, wv0, wv1, wall;
    if (!wh) {
        const u16* xp = x + (size_t)row * DM + 64 * g + 16 * g4; const u16* wp = wr + 64 * g + 16 * g4;
        xv0 = *(const u32x4*)xp; xv1 = *(const u32x4*)(xp + 8); wv0 = *(const u32x4*)wp; wv1 = *(const u32x4*)(wp + 8); wall = *(const u32x4*)(wr + l * 8);
    }
    {
        LAS float* mine = (LAS float*)(L + (wh ? 147456 : 0) + wl * 2560); LAS const float* other = (LAS const float*)(L + (wh ? 0 : 147456) + wl * 2560);
        const f32x16& os = wh ? o0 : o1;
#pragma unroll
        for (int i = 0; i < 9; ++i) mine[i * 64 + l] = os[i];
        mine[9 * 64 + l] = wh ? m0 : m1;
        LBAR();
        const f32x16& ok_ = wh ? o1 : o0; const float mk = wh ? m1 : m0;
        const float mo = other[9 * 64 + l], M = fmaxf(mk, mo);
        const float fa = __builtin_amdgcn_exp2f(mk - M), fb = __builtin_amdgcn_exp2f(mo - M);
        const float rl = __builtin_amdgcn_rcpf(ok_[8] * fa + other[8 * 64 + l] * fb);
        float r[8];
#pragma unroll
        for (int i = 0; i < 8; ++i) r[i] = (ok_[i] * fa + other[i * 64 + l] * fb) * rl;
        const u32x4 ov = {nosent(pk_bf16(r[0], r[1])), nosent(pk_bf16(r[2], r[3])), nosent(pk_bf16(r[4], r[5])), nosent(pk_bf16(r[6], r[7]))};
        ST128(ov, o_off);
    }
    STAMP(7);
    _Pragma("nounroll") for (unsigned spins = 0; spins < SPIN_CAP; ++spins) {
        if (__all(peerf == tag)) break;
        __builtin_amdgcn_s_sleep(1); CFENCE();
        peerf = LD32(WS_FLAG + ((l & 7) * 32 + rb) * 128);
    }
    CFENCE();
    u32x4 wf[8][4];
    {
        const int orow = (g4 >> 1) * 16 + 4 * (g4 & 1) + q4;
        LAS const char* bp = L + WO_L + (256 * wh + orow) * 128 + p4 * 32;
        const unsigned sw = ((orow >> 1) & 1) * 16;
#pragma unroll
        for (int ct = 0; ct < 4; ++ct)
#pragma unroll
            for (int s = 0; s < 8; ++s) wf[s][ct] = tr_frag(bp + (((ct >> 1) * 16) ^ sw) + (ct & 1) * 8 + s * 4096, 1024);
    }
    u32x4 of[8];
    const unsigned of_off = WS_O + (16 * wh + (g4 >> 1)) * 65536 + row * 32 + (g4 & 1) * 16;
    {
        unsigned pend = 15;
        _Pragma("nounroll") for (unsigned spins = 0; spins < SPIN_CAP; ++spins) {
            CFENCE();
#pragma unroll
            for (int u = 0; u < 4; ++u) if (pend & (1u << u)) { of[2 * u] = LD128(of_off + (2 * u) * 131072); of[2 * u + 1] = LD128(of_off + (2 * u + 1) * 131072); }
#pragma unroll
            for (int u = 0; u < 4; ++u) if (__all(max(umax4(of[2 * u]), umax4(of[2 * u + 1])) != SENT)) pend &= ~(1u << u);
            if (!pend) break;
        }
    }
    STAMP(8);
    f32x4 acc[4] = {};
#pragma unroll
    for (int s = 0; s < 8; ++s)
#pragma unroll
        for (int ct = 0; ct < 4; ++ct) acc[ct] = MFMA16(wf[s][ct], of[s], acc[ct]);
    LAS f32x4* redA = (LAS f32x4*)(L + wl * 2560); LAS f32x4* redB = (LAS f32x4*)(L + 157696 + wl * 1536 - 2560);
    if (wh) { redA[l] = acc[0]; redA[64 + l] = acc[1]; if (l < 32) redA[128 + l] = acc[2]; else redB[128 + l] = acc[2]; redB[192 + l] = acc[3]; }
    LBAR();
    STAMP(9);
    if (!wh) {
        float wsum = 0.f;
#pragma unroll
        for (int j = 0; j < 4; ++j) wsum = dot2(wall[j], 0x3F803F80u, wsum);
        wsum = sum_g4(row_sum(wsum));
        const f32x4 p0 = redA[l], p1 = redA[64 + l], p2 = (l < 32 ? redA : redB)[128 + l], p3 = redB[192 + l];
        float h[16]; float s1 = 0.f, s2 = 0.f, s3 = 0.f;
#pragma unroll
        for (int i = 0; i < 16; ++i) {
            const int ct = i >> 2, j = i & 3;
            const float pv = ct == 0 ? p0[j] : ct == 1 ? p1[j] : ct == 2 ? p2[j] : p3[j];
            const unsigned xw = i < 8 ? xv0[i >> 1] : xv1[(i >> 1) & 3], ww = i < 8 ? wv0[i >> 1] : wv1[(i >> 1) & 3];
            const float hv = rbf(((i & 1) ? bf_hi(xw) : bf_lo(xw)) + rbf(acc[ct][j] + pv));
            h[i] = hv; s1 += hv; s2 += hv * hv; s3 += hv * ((i & 1) ? bf_hi(ww) : bf_lo(ww));
        }
        s1 = sum_g4(s1); s2 = sum_g4(s2); s3 = sum_g4(s3);
        const unsigned stb = WS_ST + (rb * 4 + wl) * 2048 + c * 16;
        if (g4 == 0) ST128(((u32x4){nosent(__float_as_uint(s1)), nosent(__float_as_uint(s2)), nosent(__float_as_uint(s3)), 0u}), stb + g * 256);
        STAMP(10);
        u32x4 va, vb;
        _Pragma("nounroll") for (unsigned spins = 0; spins < SPIN_CAP; ++spins) {
            CFENCE();
            va = LD128(stb + g4 * 512); vb = LD128(stb + g4 * 512 + 256);
            if (__all(max(umax4(va), umax4(vb)) != SENT)) break;
        }
        STAMP(11);
        const float t1 = sum_g4(__uint_as_float(va[0]) + __uint_as_float(vb[0])), t2 = sum_g4(__uint_as_float(va[1]) + __uint_as_float(vb[1])), t3 = sum_g4(__uint_as_float(va[2]) + __uint_as_float(vb[2]));
        float mb, rstd; ln_scalars(t1, t2, mb, rstd);
        const float rr = (t3 - mb * wsum) * rstd;
        const unsigned ru = __float_as_uint(rr);
        const unsigned key = ((rr == rr ? (ru ^ (((int)ru >> 31) | 0x80000000u)) : 0u) & ~2047u) | (unsigned)(2047 - row);
        const bool best = row_umax(key) == key;
        u32x4 lnp[2], hp[2], op[2];
#pragma unroll
        for (int i = 0; i < 8; ++i) {
            const float l0 = rbf(rbf(h[2 * i] - mb) * rstd), l1 = rbf(rbf(h[2 * i + 1] - mb) * rstd);
            lnp[i >> 2][i & 3] = nosent(pk_bf16(l0, l1)); hp[i >> 2][i & 3] = nosent(pk_bf16(h[2 * i], h[2 * i + 1])); op[i >> 2][i & 3] = pk_bf16(h[2 * i] + l0, h[2 * i + 1] + l1);
        }
        const unsigned coff = (rb * 4 + wl) * 1024 + 128 * g + 32 * g4;
        const __amdgpu_buffer_rsrc_t ro = __builtin_amdgcn_make_buffer_rsrc(out, 0, SEQ * DM * 2, 0x00020000);
        const unsigned ooff = row * (DM * 2) + 128 * g + 32 * g4;
        if (best) {
            if (g == 0 && g4 == 0) ST64(((u32x2){key, nosent(ru)}), WS_CKEY + rb * 128 + wl * 8);
            ST128(lnp[0], WS_CLN + coff); ST128(lnp[1], WS_CLN + coff + 16);
            __builtin_amdgcn_raw_buffer_store_b128(op[0], ro, ooff, 0, 16); __builtin_amdgcn_raw_buffer_store_b128(op[1], ro, ooff + 16, 0, 16);
        } else {
            __builtin_amdgcn_raw_buffer_store_b128(op[0], ro, ooff, 0, 0); __builtin_amdgcn_raw_buffer_store_b128(op[1], ro, ooff + 16, 0, 0);
        }
        asm volatile("s_waitcnt vmcnt(0)" ::: "memory");
        if (best) { ST128(hp[0], WS_CH + coff); ST128(hp[1], WS_CH + coff + 16); }
        STAMP(12);
        return;
    }
    if (!(is1 || is2)) return;
    _Pragma("nounroll") for (unsigned spins = 0; spins < SPIN_CAP; ++spins) {
        CFENCE();
        const unsigned f0 = LD32(WS_FLAG + l * 128), f1 = LD32(WS_FLAG + (64 + l) * 128), f2 = LD32(WS_FLAG + (128 + l) * 128), f3 = LD32(WS_FLAG + (192 + l) * 128);
        if (__all(f0 == tag && f1 == tag && f2 == tag && f3 == tag)) break;
        __builtin_amdgcn_s_sleep(1);
    }
    CFENCE();
    LAS float* fred = (LAS float*)(L + 10240);
    const unsigned key_off = WS_CKEY + (l >> 1) * 128 + (l & 1) * 16;
    if (is1) {
        u32x4 ff[16];
        LAS const char* bp = L + FF_L + (128 * wl + brow) * 128 + (p4 & 1) * 8;
#pragma unroll
        for (int ct = 0; ct < 4; ++ct)
#pragma unroll
            for (int s = 0; s < 4; ++s) ff[s * 4 + ct] = tr_frag(bp + (((2 * ct + (p4 >> 1)) ^ bswz) * 16) + s * 4096, 512);
        u32x4 xf[4]; unsigned cur = 0;
        _Pragma("nounroll") for (unsigned spins = 0; spins < SPIN_CAP; ++spins) {
            CFENCE();
            const u32x4 kk = LD128(key_off);
            const unsigned xo = WS_CLN + ((2047 - (cur & 2047u)) >> 4) * 1024 + wl * 256 + g4 * 16;
#pragma unroll
            for (int s = 0; s < 4; ++s) xf[s] = LD128(xo + s * 64);
            const bool kval = __all(umax4(kk) != SENT);
            const unsigned nb = wave_umax(max(kk[0] == SENT ? 0u : kk[0], kk[2] == SENT ? 0u : kk[2]));
            const bool same = __builtin_amdgcn_readfirstlane(nb) == __builtin_amdgcn_readfirstlane(cur);
            cur = nb;
            if (kval && same && __all(max(max(umax4(xf[0]), umax4(xf[1])), max(umax4(xf[2]), umax4(xf[3]))) != SENT)) break;
        }
        STAMP(11);
        f32x4 a[4] = {};
#pragma unroll
        for (int s = 0; s < 4; ++s)
#pragma unroll
            for (int ct = 0; ct < 4; ++ct) a[ct] = MFMA16(ff[s * 4 + ct], xf[s], a[ct]);
        if (c == 0) {
#pragma unroll
            for (int ct = 0; ct < 4; ++ct) *(LAS f32x4*)(fred + wl * 64 + ct * 16 + 4 * g4) = a[ct];
        }
        LBAR();
        if (wl == 0 && l < 16) {
            float av[2];
#pragma unroll
            for (int j = 0; j < 2; ++j) {
                const int n = 2 * l + j;
                const float gg = rbf((fred[n] + fred[64 + n]) + (fred[128 + n] + fred[192 + n]));
                const float uu = rbf((fred[32 + n] + fred[96 + n]) + (fred[160 + n] + fred[224 + n]));
                av[j] = rbf(gg * __builtin_amdgcn_rcpf(1.f + __expf(-gg))) * uu;
            }
            ST32(nosent(pk_bf16(av[0], av[1])), WS_ACT + bid * 128 + l * 4);
        }
        STAMP(12);
    } else {
        u32x4 ff[14];
        LAS const char* bp = L + FF_L + (448 * wl + brow) * 32 + p4 * 8;
#pragma unroll
        for (int s = 0; s < 14; ++s) ff[s] = tr_frag(bp + s * 1024, 128);
        const int col0 = (bid - FFN2_BASE) * 16;
        unsigned lnv = 0, hv = 0, nb = 0; float rsel = 0.f;
        if (wl == 0) {
            u32x4 kk;
            _Pragma("nounroll") for (unsigned spins = 0; spins < SPIN_CAP; ++spins) {
                CFENCE();
                kk = LD128(key_off);
                if (__all(umax4(kk) != SENT)) break;
                __builtin_amdgcn_s_sleep(4);
            }
            nb = wave_umax(max(kk[0], kk[2]));
            rsel = __uint_as_float(wave_umax(kk[0] == nb ? kk[1] : kk[2] == nb ? kk[3] : 0u));
            const unsigned co = ((2047 - (nb & 2047u)) >> 4) * 1024 + col0 * 2 + (l & 7) * 4;
            _Pragma("nounroll") for (unsigned spins = 0; spins < SPIN_CAP; ++spins) {
                CFENCE();
                lnv = LD32(WS_CLN + co); hv = LD32(WS_CH + co);
                if (__all(max(lnv, hv) != SENT)) break;
            }
        }
        STAMP(10);
        u32x4 xf[14];
        _Pragma("nounroll") for (unsigned spins = 0; spins < SPIN_CAP; ++spins) {
            CFENCE();
            unsigned mxu = 0;
#pragma unroll
            for (int s = 0; s < 14; ++s) xf[s] = LD128(WS_ACT + (14 * wl + s) * 128 + g4 * 16);
#pragma unroll
            for (int s = 0; s < 14; ++s) mxu = max(mxu, umax4(xf[s]));
            if (__all(mxu != SENT)) break;
        }
        STAMP(11);
        f32x4 a0 = {}, a1 = {};
#pragma unroll
        for (int s = 0; s < 14; s += 2) { a0 = MFMA16(ff[s], xf[s], a0); a1 = MFMA16(ff[s + 1], xf[s + 1], a1); }
        if (c == 0) *(LAS f32x4*)(fred + wl * 16 + 4 * g4) = a0 + a1;
        LBAR();
        if (wl == 0 && l < 8) {
            const float sig = rbf(__builtin_amdgcn_rcpf(1.f + __expf(-rbf(rsel))));
            float res[2];
#pragma unroll
            for (int j = 0; j < 2; ++j) {
                const int n = 2 * l + j;
                const float y = rbf((fred[n] + fred[16 + n]) + (fred[32 + n] + fred[48 + n]));
                const float ln = j ? bf_hi(lnv) : bf_lo(lnv), hh = j ? bf_hi(hv) : bf_lo(hv);
                res[j] = hh + rbf(ln + rbf(y * sig));
            }
            const __amdgpu_buffer_rsrc_t ro = __builtin_amdgcn_make_buffer_rsrc(out, 0, SEQ * DM * 2, 0x00020000);
            __builtin_amdgcn_raw_buffer_store_b32(pk_bf16(res[0], res[1]), ro, ((2047 - (nb & 2047u)) * DM + col0) * 2 + l * 4, 0, 16);
            if (bid == FFN2_BASE && l == 0) ST32(tag, WS_EPOCH);
        }
        STAMP(12);
        if (bid == FFN2_BASE) { const u64 e = __builtin_amdgcn_s_memrealtime() + (pr_t1 - pr_t0); while (__builtin_amdgcn_s_memrealtime() < e) __builtin_amdgcn_s_sleep(1); }
    }
}

extern "C" void kernel_launch(void* const* d_in, const int* in_sizes, int n_in, void* d_out, int out_size, void* d_ws, size_t ws_size, hipStream_t stream) {
    (void)hipFuncSetAttribute((const void*)k_fused, hipFuncAttributeMaxDynamicSharedMemorySize, LDS_BYTES);
    k_fused<<<256, 512, LDS_BYTES, stream>>>((const u16*)d_in[0], (const u16*)d_in[1], (const u16*)d_in[2], (const u16*)d_in[4], (const u16*)d_in[5], (const u16*)d_in[6], (const u16*)d_in[7],
                                             (u16*)d_out, (char*)d_ws);
}
```

```cpp
#include <hip/hip_runtime.h>
#include <hip/hip_bf16.h>
#include <stdint.h>

typedef float f32x2 __attribute__((ext_vector_type(2)));
typedef float f32x4 __attribute__((ext_vector_type(4)));
typedef float f32x16 __attribute__((ext_vector_type(16)));
typedef __bf16 bf16x2 __attribute__((ext_vector_type(2)));
typedef __bf16 bf16x8 __attribute__((ext_vector_type(8)));
typedef __bf16 bf16x16 __attribute__((ext_vector_type(16)));
typedef unsigned u32x2 __attribute__((ext_vector_type(2)));
typedef unsigned u32x4 __attribute__((ext_vector_type(4)));
typedef unsigned long long u64;
typedef unsigned short u16;
#define LAS __attribute__((address_space(3)))
#define SBAR() __builtin_amdgcn_sched_barrier(0)
#define PIN(x) asm volatile("" : "+v"(x))
#define CFENCE() asm volatile("" ::: "memory")

constexpr int SEQ = 2048, DM = 512, NH = 32, FF = 1792;
constexpr unsigned KV_STRIDE = 131072 + 8192;
constexpr unsigned WS_KV = 0;
constexpr unsigned WS_O = 5u << 20;
constexpr unsigned WS_CTL = 7u << 20;
constexpr unsigned WS_FLAG = WS_CTL;
constexpr unsigned WS_EPOCH = WS_CTL + 32768;
constexpr unsigned WS_CKEY = WS_CTL + 36864;
constexpr unsigned WS_ACT = WS_CTL + 40960;
constexpr unsigned WS_ST = WS_CTL + 49152;
constexpr unsigned WS_CLN = WS_ST + 32 * 4 * 8 * 256;
constexpr unsigned WS_CH = WS_CLN + 131072;
constexpr unsigned WS_END = WS_CH + 131072;
constexpr unsigned SENT = 0xFFFFFFFFu;
constexpr unsigned SPIN_CAP = 1u << 18;
constexpr float QSCALE = 0.25f * 1.4426950408889634f;
constexpr int N_FFN1 = 56, FFN2_BASE = 64, N_FFN2 = 32;
constexpr int LDS_BYTES = 163840;

#define STAMP(i)

__device__ __forceinline__ unsigned pk_bf16(float lo, float hi) { bf16x2 v; v[0] = (__bf16)lo; v[1] = (__bf16)hi; return __builtin_bit_cast(unsigned, v); }
__device__ __forceinline__ float bf_lo(unsigned u) { return __uint_as_float(u << 16); }
__device__ __forceinline__ float bf_hi(unsigned u) { return __uint_as_float(u & 0xffff0000u); }
__device__ __forceinline__ float rbf(float x) { return (float)(__bf16)x; }
__device__ __forceinline__ unsigned nosent(unsigned u) { return u < 0xFFFFFFFEu ? u : 0xFFFFFFFEu; }
__device__ __forceinline__ unsigned umax3(unsigned a, unsigned b, unsigned c) { return max(max(a, b), c); }
__device__ __forceinline__ unsigned umax4(const u32x4& v) { return max(umax3(v[0], v[1], v[2]), v[3]); }
__device__ __forceinline__ void glds16(const void* sbase, unsigned voff, unsigned lds_base) {
    unsigned sv; asm volatile("s_mov_b32 %0, m0\n\ts_mov_b32 m0, %3\n\ts_nop 0\n\tglobal_load_lds_dwordx4 %1, %2\n\ts_mov_b32 m0, %0" : "=&s"(sv) : "v"(voff), "s"(sbase), "s"(lds_base) : "memory");
}
typedef short v4i16_t __attribute__((ext_vector_type(4)));
__device__ __forceinline__ u32x4 tr_frag(LAS const char* p, int second) {
    const u32x2 a = __builtin_bit_cast(u32x2, __builtin_amdgcn_ds_read_tr16_b64_v4i16((LAS v4i16_t*)p));
    const u32x2 b = __builtin_bit_cast(u32x2, __builtin_amdgcn_ds_read_tr16_b64_v4i16((LAS v4i16_t*)(p + second)));
    return (u32x4){a[0], a[1], b[0], b[1]};
}
__device__ __forceinline__ float x16(float v) { auto s = __builtin_amdgcn_permlane16_swap(__float_as_uint(v), __float_as_uint(v), false, false); return __uint_as_float(s[0]) + __uint_as_float(s[1]); }
__device__ __forceinline__ float x32(float v) { auto s = __builtin_amdgcn_permlane32_swap(__float_as_uint(v), __float_as_uint(v), false, false); return __uint_as_float(s[0]) + __uint_as_float(s[1]); }
__device__ __forceinline__ float sum_g4(float v) { return x32(x16(v)); }
__device__ __forceinline__ float half_max(float m) { auto s = __builtin_amdgcn_permlane32_swap(__float_as_uint(m), __float_as_uint(m), false, false); return fmaxf(__uint_as_float(s[0]), __uint_as_float(s[1])); }
#define DPP(x, ctrl) __builtin_amdgcn_update_dpp(0, (int)(x), (ctrl), 0xf, 0xf, true)
__device__ __forceinline__ unsigned row_umax(unsigned v) {
    v = max(v, (unsigned)DPP(v, 0xB1)); v = max(v, (unsigned)DPP(v, 0x4E)); v = max(v, (unsigned)DPP(v, 0x141)); v = max(v, (unsigned)DPP(v, 0x140)); return v; }
__device__ __forceinline__ float row_sum(float v) {
    v += __int_as_float(DPP(__float_as_int(v), 0xB1)); v += __int_as_float(DPP(__float_as_int(v), 0x4E)); v += __int_as_float(DPP(__float_as_int(v), 0x141)); v += __int_as_float(DPP(__float_as_int(v), 0x140)); return v; }
__device__ __forceinline__ unsigned wave_umax(unsigned v) {
    v = row_umax(v);
    auto a = __builtin_amdgcn_permlane16_swap(v, v, false, false); v = max(a[0], a[1]);
    auto b = __builtin_amdgcn_permlane32_swap(v, v, false, false); return max(b[0], b[1]); }
__device__ __forceinline__ float dot2(unsigned a, unsigned b, float c) { return __builtin_amdgcn_fdot2_f32_bf16(__builtin_bit_cast(bf16x2, a), __builtin_bit_cast(bf16x2, b), c, false); }
__device__ __forceinline__ u32x4 widen(const f32x4& a, const f32x4& b) {
    auto s0 = __builtin_amdgcn_permlane16_swap(pk_bf16(a[0], a[1]), pk_bf16(b[0], b[1]), false, false);
    auto s1 = __builtin_amdgcn_permlane16_swap(pk_bf16(a[2], a[3]), pk_bf16(b[2], b[3]), false, false);
    return (u32x4){s0[0], s1[0], s0[1], s1[1]};
}
__device__ __forceinline__ void ln_scalars(float s1, float s2, float& mb, float& rs) {
    const float m = s1 * (1.f / DM), var = fmaxf(s2 * (1.f / DM) - m * m, 0.f);
    mb = rbf(m); rs = 1.f / rbf(sqrtf(rbf(rbf(var) + 1e-5f)));
}
#define MFMA16(a, b, c) __builtin_amdgcn_mfma_f32_16x16x32_bf16(__builtin_bit_cast(bf16x8, a), __builtin_bit_cast(bf16x8, b), c, 0, 0, 0)
#define MFMA32(a, b, c) __builtin_amdgcn_mfma_f32_32x32x16_bf16(__builtin_bit_cast(bf16x8, a), __builtin_bit_cast(bf16x8, b), c, 0, 0, 0)
#define SMF(v, p, o) __builtin_amdgcn_smfmac_f32_32x32x32_bf16(__builtin_bit_cast(bf16x8, v), __builtin_bit_cast(bf16x16, p), o, sidx, 0, 0)
#define LO8(s) __builtin_shufflevector(s, s, 0, 1, 2, 3, 4, 5, 6, 7)
#define HI8(s) __builtin_shufflevector(s, s, 8, 9, 10, 11, 12, 13, 14, 15)
#define EX(s, i) s[i] = __builtin_amdgcn_exp2f(s[i])
#define LD128(off) __builtin_amdgcn_raw_buffer_load_b128(rs, (off), 0, 16)
#define LD64(off) __builtin_amdgcn_raw_buffer_load_b64(rs, (off), 0, 16)
#define LD32(off) __builtin_amdgcn_raw_buffer_load_b32(rs, (off), 0, 16)
#define ST128(v, off) __builtin_amdgcn_raw_buffer_store_b128((v), rs, (off), 0, 16)
#define ST64(v, off) __builtin_amdgcn_raw_buffer_store_b64((v), rs, (off), 0, 16)
#define ST32(v, off) __builtin_amdgcn_raw_buffer_store_b32((v), rs, (off), 0, 16)
#define WAITBAR(n) asm volatile("s_waitcnt vmcnt(" #n ") lgkmcnt(0)\n\ts_barrier" ::: "memory")
#define LBAR() asm volatile("s_waitcnt lgkmcnt(0)\n\ts_barrier" ::: "memory")

__device__ __forceinline__ float max16(const f32x16& s) {
    float a = fmaxf(fmaxf(s[0], s[1]), s[2]), b = fmaxf(fmaxf(s[3], s[4]), s[5]);
    a = fmaxf(fmaxf(a, s[6]), s[7]); b = fmaxf(fmaxf(b, s[8]), s[9]);
    a = fmaxf(fmaxf(a, s[10]), s[11]); b = fmaxf(fmaxf(b, s[12]), s[13]);
    a = fmaxf(fmaxf(a, s[14]), s[15]);
    return fmaxf(a, b);
}

__global__ __launch_bounds__(512, 2) void k_fused(const u16* __restrict__ x, const u16* __restrict__ wqkv, const u16* __restrict__ wo, const u16* __restrict__ wr,
                                                  const u16* __restrict__ wg, const u16* __restrict__ wu, const u16* __restrict__ wd, u16* __restrict__ out, char* __restrict__ ws) {
    extern __shared__ __attribute__((aligned(16))) char lds[];
    const int bid = blockIdx.x;
    const int g = (bid & 1) * 4 + ((bid >> 3) & 3), rb = ((bid >> 1) & 3) * 8 + (bid >> 5);
    const int tid = threadIdx.x, l = tid & 63, w = __builtin_amdgcn_readfirstlane(tid >> 6);
    const int c = l & 15, g4 = l >> 4, wl = w & 3, wh = w >> 2;
    const unsigned lds0 = (unsigned)(uintptr_t)lds;
    LAS char* const L = (LAS char*)lds;
    const __amdgpu_buffer_rsrc_t rs = __builtin_amdgcn_make_buffer_rsrc(ws, 0, (int)WS_END, 0x00020000);
    STAMP(0);
    unsigned code_pf;
    { u64 pc; asm volatile("s_getpc_b64 %0" : "=s"(pc)); pc &= ~127ull; const unsigned po = (tid < 176 ? tid : 0) * 128;
      asm volatile("global_load_dword %0, %1, %2" : "=v"(code_pf) : "v"(po), "s"(pc) : "memory"); }
    constexpr unsigned RING = 65536;
    const int r8 = l >> 3, s8 = l & 7;
    const unsigned xoff = (unsigned)((64 * rb + 8 * w + r8) * (DM * 2) + ((s8 ^ ((4 * w + (r8 >> 1)) & 7)) * 16));
    const unsigned woff = (unsigned)((8 * w + r8) * (3 * DM * 2) + 128 * g + ((s8 ^ ((((r8 >> 1) & 1) | ((w & 1) << 1)) << 1)) * 16));
    const char* xb = (const char*)x; const char* wb = (const char*)wqkv;
    auto dma_x = [&](int i) { glds16(xb + i * 128, xoff, lds0 + i * 8192 + w * 1024); };
    auto dma_kv = [&](int i) { const unsigned s = lds0 + RING + (i % 6) * 16384 + w * 1024; const char* p = wb + (size_t)i * 64 * 3072; glds16(p + 1024, woff, s); glds16(p + 2048, woff, s + 8192); };
    auto dma_q = [&](int j) { const unsigned s = lds0 + RING + ((j + 8) % 6) * 16384 + w * 1024; const char* p = wb + (size_t)j * 128 * 3072; glds16(p, woff, s); glds16(p + 64 * 3072, woff, s + 8192); };
#pragma unroll
    for (int i = 0; i < 6; ++i) { dma_x(i); dma_kv(i); }
    dma_x(6); dma_x(7);
    const int head = 4 * g + wl;
    const unsigned o_off = WS_O + head * 65536 + (64 * rb + 32 * wh + (l & 31)) * 32 + (l >> 5) * 16;
    {
        const u32x4 sv = {SENT, SENT, SENT, SENT};
        ST128(sv, o_off);
        if (!wh) {
            unsigned a = WS_END;
            if (l < 16) a = WS_ST + ((rb * 4 + wl) * 8 + g) * 256 + l * 16;
            else if (l < 32) a = ((l & 8) ? WS_CH : WS_CLN) + (4 * rb + wl) * 1024 + 128 * g + (l & 7) * 16;
            else if (l < 34) { if (g == 0 && wl == 0) a = WS_CKEY + rb * 128 + (l - 32) * 16; }
            else if (l < 38) { if (bid < N_FFN1 && wl == 0) a = WS_ACT + bid * 128 + (l - 34) * 16; }
            ST128(sv, a);
        }
    }
    const unsigned tag = LD32(WS_EPOCH) * 0x9E3779B1u + 0x7F4A7C15u;
    const int q4 = c >> 2, p4 = l & 3;
    const int brow = 8 * g4 + q4;
    const unsigned bswz = (((brow >> 1) & 1) | (((brow >> 3) & 1) << 1)) << 1;
    const unsigned boff = brow * 128 + (((2 * wl + (p4 >> 1)) ^ bswz) * 16) + (p4 & 1) * 8;
    const unsigned aoff0 = (32 * wh + c) * 128 + ((g4 ^ (c >> 1)) * 16), aoff1 = aoff0 ^ 64;
    u32x4 af[2][16];
    f32x4 aK0 = {}, aK1 = {}, aV0 = {}, aV1 = {}, cK = {}, cV = {};
    float s1a = 0.f, s2a = 0.f, s1b = 0.f, s2b = 0.f;
    const u32x4 ones4 = {0x3F803F80u, 0x3F803F80u, 0x3F803F80u, 0x3F803F80u};
    auto load_a = [&](int i) {
        af[0][2 * i] = *(LAS const u32x4*)(L + i * 8192 + aoff0); af[1][2 * i] = *(LAS const u32x4*)(L + i * 8192 + 2048 + aoff0);
        af[0][2 * i + 1] = *(LAS const u32x4*)(L + i * 8192 + aoff1); af[1][2 * i + 1] = *(LAS const u32x4*)(L + i * 8192 + 2048 + aoff1);
    };
    auto stat = [&](int s) {
        if ((s & 3) == wl) {
#pragma unroll
            for (int j = 0; j < 4; ++j) { s1a = dot2(af[0][s][j], 0x3F803F80u, s1a); s2a = dot2(af[0][s][j], af[0][s][j], s2a); s1b = dot2(af[1][s][j], 0x3F803F80u, s1b); s2b = dot2(af[1][s][j], af[1][s][j], s2b); }
        }
    };
    auto kv_chunk = [&](int i) {
        LAS const char* bp = L + RING + (i % 6) * 16384 + boff;
#pragma unroll
        for (int u = 0; u < 2; ++u) {
            const int s = 2 * i + u;
            const u32x4 bk = tr_frag(bp + u * 4096, 512), bv = tr_frag(bp + 8192 + u * 4096, 512);
            aK0 = MFMA16(bk, af[0][s], aK0); aK1 = MFMA16(bk, af[1][s], aK1); cK = MFMA16(bk, ones4, cK);
            aV0 = MFMA16(af[0][s], bv, aV0); aV1 = MFMA16(af[1][s], bv, aV1); cV = MFMA16(ones4, bv, cV);
        }
    };
    WAITBAR(18); asm volatile("" :: "v"(code_pf)); load_a(0); kv_chunk(0); stat(0); stat(1);
    STAMP(1);
    WAITBAR(15); dma_kv(6); load_a(1); kv_chunk(1); stat(2); stat(3);
    WAITBAR(14); dma_kv(7); load_a(2); kv_chunk(2); stat(4); stat(5);
    WAITBAR(13); dma_q(0);  load_a(3); kv_chunk(3); stat(6); stat(7);
    WAITBAR(12); dma_q(1);  load_a(4); kv_chunk(4); stat(8); stat(9);
    WAITBAR(11); dma_q(2);  load_a(5); kv_chunk(5); stat(10); stat(11);
    WAITBAR(8);  dma_q(3);  load_a(6); load_a(7); kv_chunk(6); stat(12); stat(13); stat(14); stat(15);
    {
        s1a = sum_g4(s1a); s2a = sum_g4(s2a); s1b = sum_g4(s1b); s2b = sum_g4(s2b);
        if (g4 < 2) *(LAS f32x2*)(L + (32 * wh + 16 * g4 + c) * 32 + wl * 8) = g4 ? (f32x2){s1b, s2b} : (f32x2){s1a, s2a};
    }
    WAITBAR(8);  kv_chunk(7);
    float mA, rA, mB, rB;
    {
        const f32x4 a0 = *(LAS const f32x4*)(L + (32 * wh + c) * 32), a1 = *(LAS const f32x4*)(L + (32 * wh + c) * 32 + 16);
        const f32x4 b0 = *(LAS const f32x4*)(L + (32 * wh + 16 + c) * 32), b1 = *(LAS const f32x4*)(L + (32 * wh + 16 + c) * 32 + 16);
        ln_scalars((a0[0] + a0[2]) + (a1[0] + a1[2]), (a0[1] + a0[3]) + (a1[1] + a1[3]), mA, rA);
        ln_scalars((b0[0] + b0[2]) + (b1[0] + b1[2]), (b0[1] + b0[3]) + (b1[1] + b1[3]), mB, rB);
    }
    u32x4 kown;
    {
        LAS float* sc = (LAS float*)(L + 4096 + w * 256);
        if (g4 < 2) { sc[16 * g4 + c] = g4 ? mB : mA; sc[32 + 16 * g4 + c] = g4 ? rB : rA; }
        const f32x4 mv0 = *(LAS const f32x4*)(sc + 4 * g4), mv1 = *(LAS const f32x4*)(sc + 16 + 4 * g4), rv0 = *(LAS const f32x4*)(sc + 32 + 4 * g4), rv1 = *(LAS const f32x4*)(sc + 48 + 4 * g4);
        f32x4 k0, k1, v0, v1;
#pragma unroll
        for (int j = 0; j < 4; ++j) { k0[j] = (aK0[j] - mA * cK[j]) * rA; k1[j] = (aK1[j] - mB * cK[j]) * rB; v0[j] = (aV0[j] - mv0[j] * cV[0]) * rv0[j]; v1[j] = (aV1[j] - mv1[j] * cV[0]) * rv1[j]; }
        kown = widen(k0, k1); const u32x4 vv = widen(v0, v1);
        const unsigned tb = WS_KV + head * KV_STRIDE + (2 * rb + wh) * 2048;
        ST128(kown, tb + l * 16);
        ST128(vv, tb + 1024 + (g4 & 1) * 512 + (g4 >> 1) * 256 + c * 16);
    }
    STAMP(2);
    f32x4 aQ0 = {}, aQ1 = {}, cQ = {};
    auto q_chunk = [&](int j) {
        LAS const char* bp = L + RING + ((j + 8) % 6) * 16384 + boff;
#pragma unroll
        for (int u = 0; u < 4; ++u) {
            const u32x4 bq = tr_frag(bp + u * 4096, 512);
            aQ0 = MFMA16(bq, af[0][4 * j + u], aQ0); aQ1 = MFMA16(bq, af[1][4 * j + u], aQ1); cQ = MFMA16(bq, ones4, cQ);
        }
    };
    WAITBAR(2);  q_chunk(0); q_chunk(1);
    WAITBAR(0);
    if (tid == 0) ST32(tag, WS_FLAG + (g * 32 + rb) * 128);
    STAMP(3);
    q_chunk(2); q_chunk(3);
    u32x4 q0, q1;
    {
        f32x4 t0, t1; const float ra = rA * QSCALE, rb_ = rB * QSCALE;
#pragma unroll
        for (int j = 0; j < 4; ++j) { t0[j] = (aQ0[j] - mA * cQ[j]) * ra; t1[j] = (aQ1[j] - mB * cQ[j]) * rb_; }
        const u32x4 qf = widen(t0, t1);
        *(LAS u32x4*)(L + 8192 + w * 1024 + l * 16) = qf;
        LBAR();
        q0 = *(LAS const u32x4*)(L + 8192 + wl * 1024 + l * 16); q1 = *(LAS const u32x4*)(L + 8192 + (4 + wl) * 1024 + l * 16);
    }
    const f32x16 zero16 = {};
    float m0, m1;
    { const f32x16 s0 = MFMA32(kown, q0, zero16), s1 = MFMA32(kown, q1, zero16); m0 = max16(s0); m1 = max16(s1); }
    STAMP(4);
    _Pragma("nounroll") for (unsigned spins = 0; spins < SPIN_CAP; ++spins) {
        CFENCE();
        const unsigned f = LD32(WS_FLAG + (g * 32 + 16 * wh + c) * 128);
        if (__all(f == tag)) break;
        __builtin_amdgcn_s_sleep(1);
    }
    CFENCE();
    STAMP(5);
    constexpr unsigned WO_L = 16384, FF_L = 81920, XCH_L = 147456 - 4096;
    const char* kvb = ws + WS_KV + (size_t)head * KV_STRIDE + wh * 65536;
    const unsigned koff = l * 16, voff = (l >> 5) * 256 + c * 16;
    const u64 vmask = 0x0000FFFF0000FFFFull;
    const int sidx = 0xDDDD;
    const bool is1 = bid < N_FFN1, is2 = bid >= FFN2_BASE && bid < FFN2_BASE + N_FFN2;
    const unsigned wo_off = (unsigned)((8 * w + r8) * (DM * 2) + 128 * g + ((s8 ^ ((r8 >> 1) & 1)) * 16));
    unsigned ff_off; const char* ff_base; unsigned ff_step;
    if (is1) { const int ch = s8 ^ ((((r8 >> 1) & 1) | ((w & 1) << 1)) << 1);
               ff_base = (const char*)wg; ff_off = (unsigned)((8 * w + r8) * (FF * 2) + bid * 64 + (ch & 3) * 16); ff_step = 64 * FF * 2;
               (void)ch; }
    else     { ff_base = (const char*)wd; ff_off = (unsigned)((32 * w + (l >> 1)) * (DM * 2) + (bid - FFN2_BASE) * 32 + (l & 1) * 16); ff_step = 256 * DM * 2; }
    const bool up_lane = ((s8 ^ ((((r8 >> 1) & 1) | ((w & 1) << 1)) << 1)) & 4) != 0;
    const char* ff_lane = (is1 && up_lane ? (const char*)wu : ff_base) + ff_off;
    f32x16 o0, o1;
    int nit = 8; asm volatile("" : "+s"(nit));
    const int ff_n = is1 ? 8 : is2 ? 7 : 0;
    for (int attempt = 0; attempt < 2; ++attempt) {
        if (attempt) {
            for (int t = 0; t < 32; ++t) {
                const u32x4 kf = LD128(WS_KV + head * KV_STRIDE + wh * 65536 + t * 2048 + koff);
                const f32x16 s0 = MFMA32(kf, q0, zero16), s1 = MFMA32(kf, q1, zero16);
                m0 = t ? fmaxf(m0, max16(s0)) : max16(s0); m1 = t ? fmaxf(m1, max16(s1)) : max16(s1);
            }
        }
        m0 = half_max(m0); m1 = half_max(m1);
        f32x16 n0, n1;
#pragma unroll
        for (int i = 0; i < 16; ++i) { n0[i] = -m0; n1[i] = -m1; }
        PIN(n0); PIN(n1); PIN(q0); PIN(q1);
        o0 = zero16; o1 = zero16;
        u32x4 kf0, kf1, kf2, kf3, va0, vb0, va1, vb1, va2, vb2, va3, vb3;
        const unsigned one = (l & 11) ? 0u : 0x3F803F80u;
        va0 = vb0 = va1 = vb1 = va2 = vb2 = va3 = vb3 = (u32x4){one, one, one, one};
        const char* pp = kvb + 3072;
#define LDK(kf, off) asm volatile("global_load_dwordx4 %0, %1, %2 offset:" #off " sc1" : "=v"(kf) : "v"(koff), "s"(pp) : "memory")
#define LDV(va, vb, offa, offb) asm volatile("s_mov_b64 exec, %4\n\tglobal_load_dwordx4 %0, %2, %3 offset:" #offa " sc1\n\tglobal_load_dwordx4 %1, %2, %3 offset:" #offb " sc1\n\ts_mov_b64 exec, -1" : "+v"(va), "+v"(vb) : "v"(voff), "s"(pp), "s"(vmask) : "memory")
        asm volatile("s_waitcnt vmcnt(0)" ::: "memory");
        LDK(kf0, -3072); LDV(va0, vb0, -2048, -1536); LDK(kf1, -1024); LDV(va1, vb1, 0, 512); LDK(kf2, 1024); LDV(va2, vb2, 2048, 2560); LDK(kf3, 3072);
        pp += 8192;
        asm volatile("s_waitcnt vmcnt(9)" : "+v"(kf0) :: "memory");
        f32x16 s0 = MFMA32(kf0, q0, n0), s1 = zero16;
#define HALF(SC, OC, VA, SP, OP, VPB, KN, QN, NN, MID) do { \
            EX(SC, 0); EX(SC, 1); PIN(SC); SBAR(); \
            OP = SMF(VPB, HI8(SP), OP); SBAR(); \
            EX(SC, 2); EX(SC, 3); EX(SC, 4); EX(SC, 5); EX(SC, 6); PIN(SC); SBAR(); \
            SP = MFMA32(KN, QN, NN); SBAR(); \
            MID; SBAR(); \
            EX(SC, 7); EX(SC, 8); EX(SC, 9); EX(SC, 10); EX(SC, 11); EX(SC, 12); PIN(SC); SBAR(); \
            OC = SMF(VA, LO8(SC), OC); SBAR(); \
            EX(SC, 13); EX(SC, 14); EX(SC, 15); PIN(SC); SBAR(); \
        } while (0)
#define STEP(KC, KN, VA, VB, VPA, VPB, VOFFA, VOFFB, KOFF) do { \
            HALF(s0, o0, VA, s1, o1, VPB, KC, q1, n1, { LDV(VPA, VPB, VOFFA, VOFFB); LDK(KC, KOFF); asm volatile("s_waitcnt vmcnt(9)" : "+v"(VA), "+v"(VB), "+v"(KN) :: "memory"); }); \
            HALF(s1, o1, VA, s0, o0, VB, KN, q0, n0, {}); } while (0)
        __builtin_amdgcn_s_setprio(3);
        _Pragma("nounroll") for (int it = 0; it < nit; ++it) {
            if (it == 2 + wh) __builtin_amdgcn_s_setprio(2);
            if (it == 5 + wh) __builtin_amdgcn_s_setprio(1);
            if (it == 7 + wh) __builtin_amdgcn_s_setprio(0);
            STEP(kf0, kf1, va0, vb0, va3, vb3, -4096, -3584, -3072);
            glds16((const char*)wo + it * 65536, wo_off, lds0 + WO_L + it * 8192 + w * 1024);
            if (it < ff_n) {
                unsigned sv; const unsigned dst = lds0 + FF_L + it * 8192 + w * 1024; const char* src = ff_lane + (size_t)it * ff_step;
                asm volatile("s_mov_b32 %0, m0\n\ts_mov_b32 m0, %2\n\ts_nop 0\n\tglobal_load_lds_dwordx4 %1, off\n\ts_mov_b32 m0, %0" : "=&s"(sv) : "v"(src), "s"(dst) : "memory");
            }
            STEP(kf1, kf2, va1, vb1, va0, vb0, -2048, -1536, -1024);
            STEP(kf2, kf3, va2, vb2, va1, vb1, 0, 512, 1024);
            STEP(kf3, kf0, va3, vb3, va2, vb2, 2048, 2560, 3072);
            pp += 8192;
        }
        o1 = SMF(vb3, HI8(s1), o1);
        __builtin_amdgcn_s_setprio(0);
        asm volatile("s_waitcnt vmcnt(0)" : "+v"(kf0), "+v"(kf1), "+v"(kf2), "+v"(kf3), "+v"(va0), "+v"(vb0), "+v"(va1), "+v"(vb1), "+v"(va2), "+v"(vb2) :: "memory");
        float mx = 0.f;
#pragma unroll
        for (int i = 0; i < 8; ++i) mx += fabsf(o0[i]) * 0.0625f + fabsf(o1[i]) * 0.0625f;
        const bool ok = (o0[8] <= 16777216.f) && (o1[8] <= 16777216.f) && (mx < INFINITY);
        if (__all(ok)) break;
    }
    STAMP(6);
    unsigned peerf = LD32(WS_FLAG + ((l & 7) * 32 + rb) * 128);
    const int row = 64 * rb + 16 * wl + c;
    u32x4 xv0, xv1, wv0, wv1, wall;
    if (!wh) {
        const u16* xp = x + (size_t)row * DM + 64 * g + 16 * g4; const u16* wp = wr + 64 * g + 16 * g4;
        xv0 = *(const u32x4*)xp; xv1 = *(const u32x4*)(xp + 8); wv0 = *(const u32x4*)wp; wv1 = *(const u32x4*)(wp + 8); wall = *(const u32x4*)(wr + l * 8);
    }
    {
        LAS float* mine = (LAS float*)(L + (wh ? 147456 : 0) + wl * 2560); LAS const float* other = (LAS const float*)(L + (wh ? 0 : 147456) + wl * 2560);
        const f32x16& os = wh ? o0 : o1;
#pragma unroll
        for (int i = 0; i < 9; ++i) mine[i * 64 + l] = os[i];
        mine[9 * 64 + l] = wh ? m0 : m1;
        LBAR();
        const f32x16& ok_ = wh ? o1 : o0; const float mk = wh ? m1 : m0;
        const float mo = other[9 * 64 + l], M = fmaxf(mk, mo);
        const float fa = __builtin_amdgcn_exp2f(mk - M), fb = __builtin_amdgcn_exp2f(mo - M);
        const float rl = __builtin_amdgcn_rcpf(ok_[8] * fa + other[8 * 64 + l] * fb);
        float r[8];
#pragma unroll
        for (int i = 0; i < 8; ++i) r[i] = (ok_[i] * fa + other[i * 64 + l] * fb) * rl;
        const u32x4 ov = {nosent(pk_bf16(r[0], r[1])), nosent(pk_bf16(r[2], r[3])), nosent(pk_bf16(r[4], r[5])), nosent(pk_bf16(r[6], r[7]))};
        ST128(ov, o_off);
    }
    STAMP(7);
    _Pragma("nounroll") for (unsigned spins = 0; spins < SPIN_CAP; ++spins) {
        if (__all(peerf == tag)) break;
        __builtin_amdgcn_s_sleep(1); CFENCE();
        peerf = LD32(WS_FLAG + ((l & 7) * 32 + rb) * 128);
    }
    CFENCE();
    u32x4 wf[8][4];
    {
        const int orow = (g4 >> 1) * 16 + 4 * (g4 & 1) + q4;
        LAS const char* bp = L + WO_L + (256 * wh + orow) * 128 + p4 * 32;
        const unsigned sw = ((orow >> 1) & 1) * 16;
#pragma unroll
        for (int ct = 0; ct < 4; ++ct)
#pragma unroll
            for (int s = 0; s < 8; ++s) wf[s][ct] = tr_frag(bp + (((ct >> 1) * 16) ^ sw) + (ct & 1) * 8 + s * 4096, 1024);
    }
    u32x4 of[8];
    const unsigned of_off = WS_O + (16 * wh + (g4 >> 1)) * 65536 + row * 32 + (g4 & 1) * 16;
    {
        unsigned pend = 15;
        _Pragma("nounroll") for (unsigned spins = 0; spins < SPIN_CAP; ++spins) {
            CFENCE();
#pragma unroll
            for (int u = 0; u < 4; ++u) if (pend & (1u << u)) { of[2 * u] = LD128(of_off + (2 * u) * 131072); of[2 * u + 1] = LD128(of_off + (2 * u + 1) * 131072); }
#pragma unroll
            for (int u = 0; u < 4; ++u) if (__all(max(umax4(of[2 * u]), umax4(of[2 * u + 1])) != SENT)) pend &= ~(1u << u);
            if (!pend) break;
        }
    }
    STAMP(8);
    f32x4 acc[4] = {};
#pragma unroll
    for (int s = 0; s < 8; ++s)
#pragma unroll
        for (int ct = 0; ct < 4; ++ct) acc[ct] = MFMA16(wf[s][ct], of[s], acc[ct]);
    LAS f32x4* redA = (LAS f32x4*)(L + wl * 2560); LAS f32x4* redB = (LAS f32x4*)(L + 157696 + wl * 1536 - 2560);
    if (wh) { redA[l] = acc[0]; redA[64 + l] = acc[1]; if (l < 32) redA[128 + l] = acc[2]; else redB[128 + l] = acc[2]; redB[192 + l] = acc[3]; }
    LBAR();
    STAMP(9);
    if (!wh) {
        float wsum = 0.f;
#pragma unroll
        for (int j = 0; j < 4; ++j) wsum = dot2(wall[j], 0x3F803F80u, wsum);
        wsum = sum_g4(row_sum(wsum));
        const f32x4 p0 = redA[l], p1 = redA[64 + l], p2 = (l < 32 ? redA : redB)[128 + l], p3 = redB[192 + l];
        float h[16]; float s1 = 0.f, s2 = 0.f, s3 = 0.f;
#pragma unroll
        for (int i = 0; i < 16; ++i) {
            const int ct = i >> 2, j = i & 3;
            const float pv = ct == 0 ? p0[j] : ct == 1 ? p1[j] : ct == 2 ? p2[j] : p3[j];
            const unsigned xw = i < 8 ? xv0[i >> 1] : xv1[(i >> 1) & 3], ww = i < 8 ? wv0[i >> 1] : wv1[(i >> 1) & 3];
            const float hv = rbf(((i & 1) ? bf_hi(xw) : bf_lo(xw)) + rbf(acc[ct][j] + pv));
            h[i] = hv; s1 += hv; s2 += hv * hv; s3 += hv * ((i & 1) ? bf_hi(ww) : bf_lo(ww));
        }
        s1 = sum_g4(s1); s2 = sum_g4(s2); s3 = sum_g4(s3);
        const unsigned stb = WS_ST + (rb * 4 + wl) * 2048 + c * 16;
        if (g4 == 0) ST128(((u32x4){nosent(__float_as_uint(s1)), nosent(__float_as_uint(s2)), nosent(__float_as_uint(s3)), 0u}), stb + g * 256);
        STAMP(10);
        u32x4 va, vb;
        _Pragma("nounroll") for (unsigned spins = 0; spins < SPIN_CAP; ++spins) {
            CFENCE();
            va = LD128(stb + g4 * 512); vb = LD128(stb + g4 * 512 + 256);
            if (__all(max(umax4(va), umax4(vb)) != SENT)) break;
        }
        STAMP(11);
        const float t1 = sum_g4(__uint_as_float(va[0]) + __uint_as_float(vb[0])), t2 = sum_g4(__uint_as_float(va[1]) + __uint_as_float(vb[1])), t3 = sum_g4(__uint_as_float(va[2]) + __uint_as_float(vb[2]));
        float mb, rstd; ln_scalars(t1, t2, mb, rstd);
        const float rr = (t3 - mb * wsum) * rstd;
        const unsigned ru = __float_as_uint(rr);
        const unsigned key = ((rr == rr ? (ru ^ (((int)ru >> 31) | 0x80000000u)) : 0u) & ~2047u) | (unsigned)(2047 - row);
        const bool best = row_umax(key) == key;
        u32x4 lnp[2], hp[2], op[2];
#pragma unroll
        for (int i = 0; i < 8; ++i) {
            const float l0 = rbf(rbf(h[2 * i] - mb) * rstd), l1 = rbf(rbf(h[2 * i + 1] - mb) * rstd);
            lnp[i >> 2][i & 3] = nosent(pk_bf16(l0, l1)); hp[i >> 2][i & 3] = nosent(pk_bf16(h[2 * i], h[2 * i + 1])); op[i >> 2][i & 3] = pk_bf16(h[2 * i] + l0, h[2 * i + 1] + l1);
        }
        const unsigned coff = (rb * 4 + wl) * 1024 + 128 * g + 32 * g4;
        const __amdgpu_buffer_rsrc_t ro = __builtin_amdgcn_make_buffer_rsrc(out, 0, SEQ * DM * 2, 0x00020000);
        const unsigned ooff = row * (DM * 2) + 128 * g + 32 * g4;
        if (best) {
            if (g == 0 && g4 == 0) ST64(((u32x2){key, nosent(ru)}), WS_CKEY + rb * 128 + wl * 8);
            ST128(lnp[0], WS_CLN + coff); ST128(lnp[1], WS_CLN + coff + 16);
            __builtin_amdgcn_raw_buffer_store_b128(op[0], ro, ooff, 0, 16); __builtin_amdgcn_raw_buffer_store_b128(op[1], ro, ooff + 16, 0, 16);
        } else {
            __builtin_amdgcn_raw_buffer_store_b128(op[0], ro, ooff, 0, 16); __builtin_amdgcn_raw_buffer_store_b128(op[1], ro, ooff + 16, 0, 16);
        }
        asm volatile("s_waitcnt vmcnt(0)" ::: "memory");
        if (best) { ST128(hp[0], WS_CH + coff); ST128(hp[1], WS_CH + coff + 16); }
        STAMP(12);
        return;
    }
    if (!(is1 || is2)) return;
    _Pragma("nounroll") for (unsigned spins = 0; spins < SPIN_CAP; ++spins) {
        CFENCE();
        const unsigned f0 = LD32(WS_FLAG + l * 128), f1 = LD32(WS_FLAG + (64 + l) * 128), f2 = LD32(WS_FLAG + (128 + l) * 128), f3 = LD32(WS_FLAG + (192 + l) * 128);
        if (__all(f0 == tag && f1 == tag && f2 == tag && f3 == tag)) break;
        __builtin_amdgcn_s_sleep(1);
    }
    CFENCE();
    LAS float* fred = (LAS float*)(L + 10240);
    const unsigned key_off = WS_CKEY + (l >> 1) * 128 + (l & 1) * 16;
    if (is1) {
        u32x4 ff[16];
        LAS const char* bp = L + FF_L + (128 * wl + brow) * 128 + (p4 & 1) * 8;
#pragma unroll
        for (int ct = 0; ct < 4; ++ct)
#pragma unroll
            for (int s = 0; s < 4; ++s) ff[s * 4 + ct] = tr_frag(bp + (((2 * ct + (p4 >> 1)) ^ bswz) * 16) + s * 4096, 512);
        u32x4 xf[4]; unsigned cur = 0;
        _Pragma("nounroll") for (unsigned spins = 0; spins < SPIN_CAP; ++spins) {
            CFENCE();
            const u32x4 kk = LD128(key_off);
            const unsigned xo = WS_CLN + ((2047 - (cur & 2047u)) >> 4) * 1024 + wl * 256 + g4 * 16;
#pragma unroll
            for (int s = 0; s < 4; ++s) xf[s] = LD128(xo + s * 64);
            const bool kval = __all(umax4(kk) != SENT);
            const unsigned nb = wave_umax(max(kk[0] == SENT ? 0u : kk[0], kk[2] == SENT ? 0u : kk[2]));
            const bool same = __builtin_amdgcn_readfirstlane(nb) == __builtin_amdgcn_readfirstlane(cur);
            cur = nb;
            if (kval && same && __all(max(max(umax4(xf[0]), umax4(xf[1])), max(umax4(xf[2]), umax4(xf[3]))) != SENT)) break;
        }
        STAMP(11);
        f32x4 a[4] = {};
#pragma unroll
        for (int s = 0; s < 4; ++s)
#pragma unroll
            for (int ct = 0; ct < 4; ++ct) a[ct] = MFMA16(ff[s * 4 + ct], xf[s], a[ct]);
        if (c == 0) {
#pragma unroll
            for (int ct = 0; ct < 4; ++ct) *(LAS f32x4*)(fred + wl * 64 + ct * 16 + 4 * g4) = a[ct];
        }
        LBAR();
        if (wl == 0 && l < 16) {
            float av[2];
#pragma unroll
            for (int j = 0; j < 2; ++j) {
                const int n = 2 * l + j;
                const float gg = rbf((fred[n] + fred[64 + n]) + (fred[128 + n] + fred[192 + n]));
                const float uu = rbf((fred[32 + n] + fred[96 + n]) + (fred[160 + n] + fred[224 + n]));
                av[j] = rbf(gg * __builtin_amdgcn_rcpf(1.f + __expf(-gg))) * uu;
            }
            ST32(nosent(pk_bf16(av[0], av[1])), WS_ACT + bid * 128 + l * 4);
        }
        STAMP(12);
    } else {
        u32x4 ff[14];
        LAS const char* bp = L + FF_L + (448 * wl + brow) * 32 + p4 * 8;
#pragma unroll
        for (int s = 0; s < 14; ++s) ff[s] = tr_frag(bp + s * 1024, 128);
        const int col0 = (bid - FFN2_BASE) * 16;
        unsigned lnv = 0, hv = 0, nb = 0; float rsel = 0.f;
        if (wl == 0) {
            u32x4 kk;
            _Pragma("nounroll") for (unsigned spins = 0; spins < SPIN_CAP; ++spins) {
                CFENCE();
                kk = LD128(key_off);
                if (__all(umax4(kk) != SENT)) break;
                __builtin_amdgcn_s_sleep(4);
            }
            nb = wave_umax(max(kk[0], kk[2]));
            rsel = __uint_as_float(wave_umax(kk[0] == nb ? kk[1] : kk[2] == nb ? kk[3] : 0u));
            const unsigned co = ((2047 - (nb & 2047u)) >> 4) * 1024 + col0 * 2 + (l & 7) * 4;
            _Pragma("nounroll") for (unsigned spins = 0; spins < SPIN_CAP; ++spins) {
                CFENCE();
                lnv = LD32(WS_CLN + co); hv = LD32(WS_CH + co);
                if (__all(max(lnv, hv) != SENT)) break;
            }
        }
        STAMP(10);
        u32x4 xf[14];
        _Pragma("nounroll") for (unsigned spins = 0; spins < SPIN_CAP; ++spins) {
            CFENCE();
            unsigned mxu = 0;
#pragma unroll
            for (int s = 0; s < 14; ++s) xf[s] = LD128(WS_ACT + (14 * wl + s) * 128 + g4 * 16);
#pragma unroll
            for (int s = 0; s < 14; ++s) mxu = max(mxu, umax4(xf[s]));
            if (__all(mxu != SENT)) break;
        }
        STAMP(11);
        f32x4 a0 = {}, a1 = {};
#pragma unroll
        for (int s = 0; s < 14; s += 2) { a0 = MFMA16(ff[s], xf[s], a0); a1 = MFMA16(ff[s + 1], xf[s + 1], a1); }
        if (c == 0) *(LAS f32x4*)(fred + wl * 16 + 4 * g4) = a0 + a1;
        LBAR();
        if (wl == 0 && l < 8) {
            const float sig = rbf(__builtin_amdgcn_rcpf(1.f + __expf(-rbf(rsel))));
            float res[2];
#pragma unroll
            for (int j = 0; j < 2; ++j) {
                const int n = 2 * l + j;
                const float y = rbf((fred[n] + fred[16 + n]) + (fred[32 + n] + fred[48 + n]));
                const float ln = j ? bf_hi(lnv) : bf_lo(lnv), hh = j ? bf_hi(hv) : bf_lo(hv);
                res[j] = hh + rbf(ln + rbf(y * sig));
            }
            const __amdgpu_buffer_rsrc_t ro = __builtin_amdgcn_make_buffer_rsrc(out, 0, SEQ * DM * 2, 0x00020000);
            __builtin_amdgcn_raw_buffer_store_b32(pk_bf16(res[0], res[1]), ro, ((2047 - (nb & 2047u)) * DM + col0) * 2 + l * 4, 0, 16);
            if (bid == FFN2_BASE && l == 0) ST32(tag, WS_EPOCH);
        }
        STAMP(12);
    }
}

extern "C" void kernel_launch(void* const* d_in, const int* in_sizes, int n_in, void* d_out, int out_size, void* d_ws, size_t ws_size, hipStream_t stream) {
    (void)hipFuncSetAttribute((const void*)k_fused, hipFuncAttributeMaxDynamicSharedMemorySize, LDS_BYTES);
    k_fused<<<256, 512, LDS_BYTES, stream>>>((const u16*)d_in[0], (const u16*)d_in[1], (const u16*)d_in[2], (const u16*)d_in[4], (const u16*)d_in[5], (const u16*)d_in[6], (const u16*)d_in[7],
                                             (u16*)d_out, (char*)d_ws);
}
```

```cpp
#include <hip/hip_runtime.h>
#include <hip/hip_bf16.h>
#include <stdint.h>

typedef float f32x4 __attribute__((ext_vector_type(4)));
typedef float f32x16 __attribute__((ext_vector_type(16)));
typedef __bf16 bf16x2 __attribute__((ext_vector_type(2)));
typedef __bf16 bf16x8 __attribute__((ext_vector_type(8)));
typedef unsigned u32x2 __attribute__((ext_vector_type(2)));
typedef unsigned u32x4 __attribute__((ext_vector_type(4)));
typedef unsigned long long u64;
typedef unsigned short u16;
#define LAS __attribute__((address_space(3)))
#define SBAR() __builtin_amdgcn_sched_barrier(0)
#define PIN(x) asm volatile("" : "+v"(x))
#define CFENCE() asm volatile("" ::: "memory")

constexpr int SEQ = 2048, DM = 512, NH = 32, FF = 1792;
constexpr unsigned KV_STRIDE = 131072;
constexpr unsigned WS_KV = 0;
constexpr unsigned WS_O = 5u << 20;
constexpr unsigned WS_CTL = 7u << 20;
constexpr unsigned WS_FLAG = WS_CTL;
constexpr unsigned WS_EPOCH = WS_CTL + 32768;
constexpr unsigned WS_CKEY = WS_CTL + 36864;
constexpr unsigned WS_YP = WS_CTL + 40960;
constexpr unsigned WS_ST = WS_YP + 56 * 2048;
constexpr unsigned WS_CLN = WS_ST + 32 * 4 * 8 * 256;
constexpr unsigned WS_CH = WS_CLN + 131072;
constexpr unsigned WS_KVF = WS_CH + 131072;
constexpr unsigned WS_FLAG3 = WS_KVF + 131072;
constexpr unsigned WS_END = WS_FLAG3 + 32768;
constexpr unsigned SENT = 0xFFFFFFFFu;
constexpr unsigned SPIN_CAP = 1u << 18;
constexpr float QSCALE = 0.25f * 1.4426950408889634f;
constexpr int N_FFN1 = 56, FFN2_BASE = 64, N_FFN2 = 16;
constexpr int LDS_BYTES = 163840;
constexpr int XG = 2, XG3 = 8;
constexpr float L_MAX = 1.2676506e30f;

__device__ __forceinline__ unsigned pk_bf16(float lo, float hi) { bf16x2 v; v[0] = (__bf16)lo; v[1] = (__bf16)hi; return __builtin_bit_cast(unsigned, v); }
__device__ __forceinline__ unsigned pk_keep(float lo, float hi) { unsigned r = pk_bf16(lo, hi); asm("" : "+v"(r)); return r; }
__device__ __forceinline__ float bf_lo(unsigned u) { return __uint_as_float(u << 16); }
__device__ __forceinline__ float bf_hi(unsigned u) { return __uint_as_float(u & 0xffff0000u); }
__device__ __forceinline__ float rbf(float x) { return (float)(__bf16)x; }
__device__ __forceinline__ unsigned nosent(unsigned u) { return u < 0xFFFFFFFEu ? u : 0xFFFFFFFEu; }
__device__ __forceinline__ unsigned umax3(unsigned a, unsigned b, unsigned c) { return max(max(a, b), c); }
__device__ __forceinline__ unsigned umax4(const u32x4& v) { return max(umax3(v[0], v[1], v[2]), v[3]); }
#define GLDS2(sbase, va, vb, lds_base, off, goff) asm volatile("s_add_u32 m0, %3, %4\n\ts_nop 0\n\tglobal_load_lds_dwordx4 %0, %2 offset:%5\n\ts_add_u32 m0, m0, 1024\n\ts_nop 0\n\tglobal_load_lds_dwordx4 %1, %2 offset:%5" \
        :: "v"(va), "v"(vb), "s"(sbase), "s"(lds_base), "i"((off) - (goff)), "i"(goff) : "memory", "scc")
typedef short v4i16_t __attribute__((ext_vector_type(4)));
__device__ __forceinline__ u32x4 tr_frag(LAS const char* p, int second) {
    const u32x2 a = __builtin_bit_cast(u32x2, __builtin_amdgcn_ds_read_tr16_b64_v4i16((LAS v4i16_t*)p));
    const u32x2 b = __builtin_bit_cast(u32x2, __builtin_amdgcn_ds_read_tr16_b64_v4i16((LAS v4i16_t*)(p + second)));
    return (u32x4){a[0], a[1], b[0], b[1]};
}
__device__ __forceinline__ float x16(float v) { auto s = __builtin_amdgcn_permlane16_swap(__float_as_uint(v), __float_as_uint(v), false, false); return __uint_as_float(s[0]) + __uint_as_float(s[1]); }
__device__ __forceinline__ float x32(float v) { auto s = __builtin_amdgcn_permlane32_swap(__float_as_uint(v), __float_as_uint(v), false, false); return __uint_as_float(s[0]) + __uint_as_float(s[1]); }
__device__ __forceinline__ float sum_g4(float v) { return x32(x16(v)); }
#define DPP(x, ctrl) __builtin_amdgcn_update_dpp(0, (int)(x), (ctrl), 0xf, 0xf, true)
__device__ __forceinline__ unsigned row_umax(unsigned v) {
    v = max(v, (unsigned)DPP(v, 0xB1)); v = max(v, (unsigned)DPP(v, 0x4E)); v = max(v, (unsigned)DPP(v, 0x141)); v = max(v, (unsigned)DPP(v, 0x140)); return v; }
__device__ __forceinline__ float row_sum(float v) {
    v += __int_as_float(DPP(__float_as_int(v), 0xB1)); v += __int_as_float(DPP(__float_as_int(v), 0x4E)); v += __int_as_float(DPP(__float_as_int(v), 0x141)); v += __int_as_float(DPP(__float_as_int(v), 0x140)); return v; }
__device__ __forceinline__ unsigned wave_umax(unsigned v) {
    v = row_umax(v);
    auto a = __builtin_amdgcn_permlane16_swap(v, v, false, false); v = max(a[0], a[1]);
    auto b = __builtin_amdgcn_permlane32_swap(v, v, false, false); return max(b[0], b[1]); }
__device__ __forceinline__ float dot2(unsigned a, unsigned b, float c) { return __builtin_amdgcn_fdot2_f32_bf16(__builtin_bit_cast(bf16x2, a), __builtin_bit_cast(bf16x2, b), c, false); }
__device__ __forceinline__ u32x4 widen(const f32x4& a, const f32x4& b) {
    auto s0 = __builtin_amdgcn_permlane16_swap(pk_bf16(a[0], a[1]), pk_bf16(b[0], b[1]), false, false);
    auto s1 = __builtin_amdgcn_permlane16_swap(pk_bf16(a[2], a[3]), pk_bf16(b[2], b[3]), false, false);
    return (u32x4){s0[0], s1[0], s0[1], s1[1]};
}
__device__ __forceinline__ u32x4 ld2x8(const char* p) { const u32x2 a = *(const u32x2*)p, b = *(const u32x2*)(p + 32); return (u32x4){a[0], a[1], b[0], b[1]}; }
__device__ __forceinline__ u32x4 widen_pk(const u32x4& v) {
    auto s0 = __builtin_amdgcn_permlane16_swap(v[0], v[2], false, false); auto s1 = __builtin_amdgcn_permlane16_swap(v[1], v[3], false, false);
    return (u32x4){s0[0], s1[0], s0[1], s1[1]};
}
__device__ __forceinline__ float sel4(int g4, float a, float b, float c, float d) { PIN(a); PIN(b); PIN(c); PIN(d); const float t0 = (g4 & 1) ? b : a, t1 = (g4 & 1) ? d : c; return (g4 & 2) ? t1 : t0; }
__device__ __forceinline__ unsigned best_key(const u32x4& kk) { const unsigned m = (unsigned)__builtin_amdgcn_readfirstlane(wave_umax(max(kk[0] + 1u, kk[2] + 1u))); return m ? m - 1u : 0u; }
__device__ __forceinline__ void ln_scalars(float s1, float s2, float& mb, float& rs) {
    const float m = s1 * (1.f / DM), var = fmaxf(s2 * (1.f / DM) - m * m, 0.f);
    mb = rbf(m); rs = __builtin_amdgcn_rcpf(rbf(__builtin_amdgcn_sqrtf(rbf(rbf(var) + 1e-5f))));
}
#define MFMA16(a, b, c) __builtin_amdgcn_mfma_f32_16x16x32_bf16(__builtin_bit_cast(bf16x8, a), __builtin_bit_cast(bf16x8, b), c, 0, 0, 0)
#define MFMA32(a, b, c) __builtin_amdgcn_mfma_f32_32x32x16_bf16(__builtin_bit_cast(bf16x8, a), __builtin_bit_cast(bf16x8, b), c, 0, 0, 0)
#define LD128(off) __builtin_amdgcn_raw_buffer_load_b128(rs, (off), 0, 16)
#define LD32(off) __builtin_amdgcn_raw_buffer_load_b32(rs, (off), 0, 16)
#define ST128(v, off) __builtin_amdgcn_raw_buffer_store_b128((v), rs, (off), 0, 16)
#define ST64(v, off) __builtin_amdgcn_raw_buffer_store_b64((v), rs, (off), 0, 16)
#define ST32(v, off) __builtin_amdgcn_raw_buffer_store_b32((v), rs, (off), 0, 16)
#define WAITBAR(n) do { __builtin_amdgcn_s_waitcnt(0xC07F); asm volatile("s_waitcnt vmcnt(" #n ") lgkmcnt(0)\n\ts_barrier" ::: "memory"); } while (0)
#define LBAR() asm volatile("s_waitcnt lgkmcnt(0)\n\ts_barrier" ::: "memory")

__device__ __forceinline__ float max16(const f32x16& s) {
    float a = fmaxf(fmaxf(s[0], s[1]), s[2]), b = fmaxf(fmaxf(s[3], s[4]), s[5]);
    a = fmaxf(fmaxf(a, s[6]), s[7]); b = fmaxf(fmaxf(b, s[8]), s[9]);
    a = fmaxf(fmaxf(a, s[10]), s[11]); b = fmaxf(fmaxf(b, s[12]), s[13]);
    a = fmaxf(fmaxf(a, s[14]), s[15]);
    return fmaxf(a, b);
}

__global__ __launch_bounds__(512, 2) void k_fused(const u16* __restrict__ x, const u16* __restrict__ wqkv, const u16* __restrict__ wo, const u16* __restrict__ wr,
                                                  const u16* __restrict__ wg, const u16* __restrict__ wu, const u16* __restrict__ wd, u16* __restrict__ out, char* __restrict__ ws) {
    extern __shared__ __attribute__((aligned(16))) char lds[];
    const int bid = blockIdx.x;
    const int g = ((bid & 7) % (8 / XG)) * XG + ((bid >> 3) % XG), rb = ((bid & 7) / (8 / XG)) * (32 / XG) + (bid >> 3) / XG;
    const int g3 = ((bid & 7) % (8 / XG3)) * XG3 + ((bid >> 3) % XG3), rb3 = ((bid & 7) / (8 / XG3)) * (32 / XG3) + (bid >> 3) / XG3;
    const int tid = threadIdx.x, l = tid & 63, w = __builtin_amdgcn_readfirstlane(tid >> 6);
    const int c = l & 15, g4 = l >> 4, wl = w & 3, wh = w >> 2;
    const unsigned lds0 = (unsigned)(uintptr_t)lds;
    LAS char* const L = (LAS char*)lds;
    const __amdgpu_buffer_rsrc_t rs = __builtin_amdgcn_make_buffer_rsrc(ws, 0, (int)WS_END, 0x00020000);
    constexpr unsigned RING = 65536, UP_L = 147456, SCAL = RING + 7 * 8192;
    const int r8 = l >> 3, s8 = l & 7, q4 = c >> 2, p4 = l & 3;
    const int rot = (rb + (8 / XG) * g) & 7;
    const unsigned xa = (unsigned)((64 * rb + 16 * wl + r8) * (DM * 2) + ((s8 ^ (r8 >> 1)) * 16)), xb = (xa ^ 64) + 8 * DM * 2;
    const unsigned wa = (unsigned)((16 * wl + r8) * (3 * DM * 2) + 128 * g + ((s8 ^ (((r8 >> 1) & 1) << 1)) * 16)), wb = (wa ^ 64) + 8 * 3 * DM * 2;
    const unsigned ldsw = lds0 + wl * 2048;
#define XP(i) ((const char*)x + (((i) + rot) & 7) * 128)
#define WP(i) ((const char*)wqkv + (((i) + rot) & 7) * (64 * 3072))
#define QPIECE(i) ((i) < 3 ? 4 + (i) : (i) < 7 ? 5 + (i) : 0)
#define dma_x(i, P) GLDS2(P, xa, xb, ldsw, (i) * 8192, 0)
#define dma_kv(i, P) do { GLDS2(P, wa, wb, ldsw, RING + ((i) % 6) * 16384, 1024); GLDS2(P, wa, wb, ldsw, RING + ((i) % 6) * 16384 + 8192, 2048); } while (0)
#define dma_q(i, P) GLDS2(P, wa, wb, ldsw, RING + QPIECE(i) * 8192, 0)
    LAS float* const sc = (LAS float*)(L + SCAL);
    const int head = 4 * g + wl;
    const int fid = (bid >> 4) * 16 + (bid & 7) * 2 + ((bid >> 3) & 1);
    const bool is1 = fid < N_FFN1, is2 = bid >= FFN2_BASE && bid < FFN2_BASE + N_FFN2;
    unsigned code_pf, wq_pf, tagv; u32x4 wall;
    if (wh) {
        __builtin_amdgcn_s_setprio(2);
        { const char* a = XP(0); const char* b = WP(0); dma_x(0, a); dma_kv(0, b); }
        { const char* a = XP(1); const char* b = WP(1); dma_x(1, a); dma_kv(1, b); }
    } else {
        u64 pc; int len; asm volatile("s_getpc_b64 %0\n.Lkf_pc:\n\ts_mov_b32 %1, .Lkf_end-.Lkf_pc" : "=s"(pc), "=s"(len));
        const unsigned po = min((unsigned)((bid >> 3) + 32 * (2 * wl + (l & 1))) * 128u, (unsigned)max(len - 4, 0));
        asm volatile("global_load_dword %0, %4, %5\n\tglobal_load_dword %1, %6, %7\n\tglobal_load_dword %2, %8, %9 sc1\n\tglobal_load_dwordx4 %3, %10, %11"
                     : "=&v"(code_pf), "=&v"(wq_pf), "=&v"(tagv), "=&v"(wall)
                     : "v"(po), "s"(pc), "v"((unsigned)(((rb & 7) * 64 + 16 * wl + c) * 3072 + 128 * g)), "s"(wqkv), "v"(WS_EPOCH), "s"(ws), "v"(l * 16), "s"(wr) : "memory");
    }
    SBAR();
    unsigned is1i; asm("s_lshr_b32 %0, %1, 31" : "=s"(is1i) : "s"(fid - N_FFN1) : "scc");
    constexpr unsigned WO_L = 0, SC_L = 65536, FF_L = 81920;
    const char* kvb = ws + WS_KV + (size_t)head * KV_STRIDE + wh * 65536;
    unsigned koff = l * 16, voff = (l >> 5) * 256 + c * 16;
    unsigned one = (l & 11) ? 0u : 0x3F803F80u;
    unsigned wo_off = (unsigned)((8 * w + r8) * (DM * 2) + 128 * g3 + ((s8 ^ (((r8 >> 1) & 3) << 1)) * 16));
    unsigned gu_off = (unsigned)((16 * w + (l >> 2)) * (FF * 2) + (is1 ? fid : 0) * 64 + p4 * 16);
    unsigned wd_off = (unsigned)(((is1 ? fid : 0) * 32 + r8) * (DM * 2) + 128 * w + s8 * 16);
    unsigned pf_dst = lds0 + WO_L + w * 1024;
    const unsigned sched = wh ? 0x1522633Fu : 0x41522E33u;
    unsigned kvf_off = WS_KVF + (head * 32 + 16 * wh + c) * 128, pf_off = WS_FLAG + (r8 * 32 + rb3) * 128 + p4 * 4;
    unsigned pf_off2 = WS_FLAG3 + ((w == 4 && (is1 || is2)) ? (l & 31) * 128 : (r8 * 32 + rb3) * 128 + p4 * 4);
    unsigned clr_f = (l > 1 || !wh) ? WS_END : (l ? WS_FLAG3 + (g3 * 32 + rb3) * 128 : WS_FLAG + (g * 32 + rb) * 128) + wl * 4;
    unsigned tch;
    {
        const int r2 = (bid >> 3) * 16 + 2 * w + (l & 1);
        const int xg = bid & 7, k = (l >> 1) & 3, pr = min(xg + 8 * k, 24 + (xg & 3));
        tch = (l & 8) ? pr * 65536 + r2 * 128 : r2 * (FF * 2) + pr * 128;
    }
    unsigned kv_start = (unsigned)(rb & 1) * 32768u;
    unsigned of_off = WS_O + (16 * wh + (g4 >> 1)) * 65536 + (64 * rb3 + 16 * wl + c) * 32 + (g4 & 1) * 16;
    unsigned o_off = WS_O + head * 65536 + (64 * rb + 32 * wh + (l & 31)) * 32 + (l >> 5) * 16;
    unsigned mine = SC_L + w * 2048 + l * 16, minel = UP_L + 14336 + w * 256 + l * 4;
    unsigned xq = RING + 16384 + wl * 3072 + l * 16;
    unsigned xw_off = (unsigned)(64 * g3 + 32 * wh + 4 * g4) * 2, xr_off = (unsigned)(64 * rb3 + 16 * wl + c) * (DM * 2) + xw_off;
    PIN(koff); PIN(voff); PIN(one); PIN(wo_off); PIN(gu_off); PIN(wd_off); PIN(kvf_off); PIN(pf_off); PIN(pf_off2); PIN(clr_f); PIN(tch); PIN(o_off); PIN(of_off); PIN(mine); PIN(minel); PIN(xq); PIN(xw_off); PIN(xr_off);
    asm volatile("" : "+s"(kvb), "+s"(pf_dst), "+s"(kv_start));
    LAS unsigned* const arr = (LAS unsigned*)(L + UP_L + 13312);
    unsigned tag;
    u32x4 q0, q1, kown, xv, wv; float wsum;
    if (__builtin_expect(wh, 0)) {
        const char* xp[8]; const char* wp[8];
#pragma unroll
        for (int i = 0; i < 8; ++i) { xp[i] = XP(i); wp[i] = WP(i); asm volatile("" : "+s"(xp[i]), "+s"(wp[i])); }
        unsigned so0 = wl * 2048 + c * 128 + ((g4 ^ (c >> 1)) * 16), so1 = so0 ^ 64; PIN(so0); PIN(so1);
        u32x4 on4 = {0x3F803F80u, 0x3F803F80u, 0x3F803F80u, 0x3F803F80u}; PIN(on4);
        f32x4 sq = {}, sm = {};
        auto stat = [&](int i) {
            const u32x4 a = *(LAS const u32x4*)(L + so0 + i * 8192), b = *(LAS const u32x4*)(L + so1 + i * 8192);
            sq = MFMA16(a, a, sq); sm = MFMA16(on4, a, sm); sq = MFMA16(b, b, sq); sm = MFMA16(on4, b, sm); PIN(sq); PIN(sm); SBAR();
        };
        WAITBAR(6);  dma_x(2, xp[2]); dma_kv(2, wp[2]); stat(0);
        WAITBAR(6);  dma_x(3, xp[3]); dma_kv(3, wp[3]); stat(1); dma_x(4, xp[4]); dma_kv(4, wp[4]);
        WAITBAR(6);  dma_x(5, xp[5]); dma_kv(5, wp[5]); stat(2); stat(3); dma_x(6, xp[6]); dma_x(7, xp[7]); dma_kv(6, wp[6]);
        WAITBAR(8);  dma_kv(7, wp[7]); stat(4); stat(5);
        asm volatile("s_waitcnt vmcnt(8)" ::: "memory");
        stat(6); stat(7);
        dma_q(0, wp[0]); dma_q(1, wp[1]); dma_q(2, wp[2]);
        {
            float mm, rr;
            ln_scalars(sm[0], p4 == 0 ? sq[0] : p4 == 1 ? sq[1] : p4 == 2 ? sq[2] : sq[3], mm, rr);
            if (q4 == g4) { sc[16 * wl + c] = mm; sc[64 + 16 * wl + c] = rr; }
        }
        WAITBAR(6);  dma_q(3, wp[3]); dma_q(4, wp[4]); dma_q(5, wp[5]); dma_q(6, wp[6]);
        LBAR();      dma_q(7, wp[7]);
        unsigned clr_a = WS_END;
        if (l < 16) clr_a = WS_ST + ((rb3 * 4 + wl) * 8 + g3) * 256 + l * 16;
        else if (l < 32) clr_a = ((l & 8) ? WS_CH : WS_CLN) + (4 * rb3 + wl) * 1024 + 128 * g3 + s8 * 16;
        else if (is1) clr_a = WS_YP + fid * 2048 + wl * 512 + (l - 32) * 16;
        PIN(clr_a);
        tag = LD32(WS_EPOCH);
        xv = ld2x8((const char*)x + xr_off); wv = ld2x8((const char*)wr + xw_off);
        tag = __builtin_amdgcn_readfirstlane(tag) * 0x9E3779B1u + 0x7F4A7C15u;
        wsum = 0.f; PIN(xv); PIN(wv); asm volatile("" : "+s"(tag));
        WAITBAR(0);
        __builtin_amdgcn_s_setprio(0);
        {
            const u32x4 sv = {SENT, SENT, SENT, SENT};
            ST128(sv, o_off); ST128(sv, o_off - 1024); ST128(sv, clr_a); ST128(sv, (g3 == 0 && w == 4 && l < 2) ? WS_CKEY + rb3 * 128 + l * 16 : WS_END);
        }
        LBAR();
        if (tid == 256) arr[4] = 0u;
        q1 = *(LAS const u32x4*)(L + xq); q0 = *(LAS const u32x4*)(L + xq + 1024); kown = *(LAS const u32x4*)(L + xq + 2048);
        asm volatile(".Lkf_end:");
    } else {
        const int brow = 8 * g4 + q4;
        unsigned bo0 = RING + brow * 128 + (((2 * wl + (p4 >> 1)) ^ ((((brow >> 1) & 1) | (((brow >> 3) & 1) << 1)) << 1)) * 16) + (p4 & 1) * 8, bo1 = bo0 + 49152;
        unsigned ao0 = c * 128 + ((g4 ^ (c >> 1)) * 16), ao1 = ao0 ^ 64;
        u32x4 ones4 = {0x3F803F80u, 0x3F803F80u, 0x3F803F80u, 0x3F803F80u};
        unsigned st_k = WS_KV + head * KV_STRIDE + rb * 4096 + l * 16, st_v = WS_KV + head * KV_STRIDE + rb * 4096 + 1024 + (g4 & 1) * 512 + (g4 >> 1) * 256 + c * 16;
        unsigned st_f = l ? WS_END : WS_KVF + (head * 32 + rb) * 128, sca = SCAL + c * 4, scb = SCAL + g4 * 16;
        PIN(bo0); PIN(bo1); PIN(ao0); PIN(ao1); PIN(ones4); PIN(st_k); PIN(st_v); PIN(st_f); PIN(sca); PIN(scb);
#define BFRAG(piece, u) tr_frag(L + ((piece) < 6 ? bo0 : bo1) + ((piece) % 6) * 8192 + (u) * 4096, 512)
#define AFRAG(i, u, t) (*(LAS const u32x4*)(L + ((u) ? ao1 : ao0) + (i) * 8192 + (t) * 2048))
        u32x4 fa[2][2][4], fb[2][2][2];
        f32x4 aK[4] = {}, aV[4] = {}, cK = {}, cV = {};
        auto rd_a = [&](int i, int u, int t) { fa[i & 1][u][t] = AFRAG(i, u, t); fa[i & 1][u][t + 1] = AFRAG(i, u, t + 1); SBAR(); };
        auto rd_b = [&](int i, int u, int m) { fb[i & 1][u][m] = BFRAG(2 * (i % 6) + m, u); SBAR(); };
        auto mk = [&](int i, int u, int t) { aK[t] = MFMA16(fb[i & 1][u][0], fa[i & 1][u][t], aK[t]); aK[t + 1] = MFMA16(fb[i & 1][u][0], fa[i & 1][u][t + 1], aK[t + 1]); SBAR(); };
        auto mv = [&](int i, int u, int t) { aV[t] = MFMA16(fa[i & 1][u][t], fb[i & 1][u][1], aV[t]); aV[t + 1] = MFMA16(fa[i & 1][u][t + 1], fb[i & 1][u][1], aV[t + 1]); SBAR(); };
        auto mc = [&](int i, int u) { cK = MFMA16(fb[i & 1][u][0], ones4, cK); cV = MFMA16(ones4, fb[i & 1][u][1], cV); PIN(cK); PIN(cV); SBAR(); };
#define KV_RD(i) do { rd_b(i, 0, 0); rd_a(i, 0, 0); rd_a(i, 0, 2); rd_b(i, 0, 1); rd_b(i, 1, 0); rd_a(i, 1, 0); rd_a(i, 1, 2); rd_b(i, 1, 1); } while (0)
#define KV_MM(i) do { mk(i, 0, 0); mk(i, 0, 2); mv(i, 0, 0); mv(i, 0, 2); mc(i, 0); mk(i, 1, 0); mk(i, 1, 2); mv(i, 1, 0); mv(i, 1, 2); mc(i, 1); } while (0)
#define KV_IT(i) do { mk(i - 1, 0, 0); rd_b(i, 0, 0); mk(i - 1, 0, 2); rd_a(i, 0, 0); mv(i - 1, 0, 0); rd_a(i, 0, 2); mv(i - 1, 0, 2); rd_b(i, 0, 1); mc(i - 1, 0); rd_b(i, 1, 0); \
                      mk(i - 1, 1, 0); rd_a(i, 1, 0); mk(i - 1, 1, 2); rd_a(i, 1, 2); mv(i - 1, 1, 0); rd_b(i, 1, 1); mv(i - 1, 1, 2); mc(i - 1, 1); } while (0)
        WAITBAR(63); KV_RD(0);
        asm volatile("s_waitcnt vmcnt(0)" : "+v"(code_pf), "+v"(wq_pf), "+v"(tagv), "+v"(wall) :: "memory");
        wsum = 0.f;
#pragma unroll
        for (int j = 0; j < 4; ++j) wsum = dot2(wall[j], 0x3F803F80u, wsum);
        wsum = sum_g4(row_sum(wsum)); PIN(wsum);
        tag = __builtin_amdgcn_readfirstlane(tagv) * 0x9E3779B1u + 0x7F4A7C15u;
        WAITBAR(63); KV_IT(1); KV_MM(1);
        WAITBAR(63); KV_RD(2); KV_IT(3);
        WAITBAR(63); KV_IT(4); KV_IT(5);
        WAITBAR(63); KV_IT(6);
        WAITBAR(63); KV_IT(7);
        float mt[4], rt[4]; f32x4 mv4[4], rv4[4];
#pragma unroll
        for (int t = 0; t < 4; ++t) { mt[t] = *(LAS const float*)(L + sca + 64 * t); rt[t] = *(LAS const float*)(L + sca + 256 + 64 * t); }
        SBAR();
        mk(7, 0, 0); mk(7, 0, 2); cK = MFMA16(fb[1][0][0], ones4, cK); mk(7, 1, 0); mk(7, 1, 2); cK = MFMA16(fb[1][1][0], ones4, cK); SBAR();
#pragma unroll
        for (int t = 0; t < 4; ++t) { mv4[t] = *(LAS const f32x4*)(L + scb + 64 * t); rv4[t] = *(LAS const f32x4*)(L + scb + 256 + 64 * t); }
        cV = MFMA16(ones4, fb[1][0][1], cV); mv(7, 0, 0); mv(7, 0, 2); cV = MFMA16(ones4, fb[1][1][1], cV); mv(7, 1, 0); mv(7, 1, 2);
        f32x4 kk[4], vv[4];
#pragma unroll
        for (int t = 0; t < 4; ++t)
#pragma unroll
            for (int j = 0; j < 4; ++j) kk[t][j] = (aK[t][j] - mt[t] * cK[j]) * rt[t];
        const u32x4 k0 = widen(kk[0], kk[1]); kown = widen(kk[2], kk[3]);
        __builtin_amdgcn_raw_buffer_store_b128(k0, rs, st_k, 0, 16); __builtin_amdgcn_raw_buffer_store_b128(kown, rs, st_k + 2048, 0, 16);
        SBAR();
        rd_a(0, 0, 0); rd_a(0, 0, 2); rd_a(0, 1, 0); rd_a(0, 1, 2);
#pragma unroll
        for (int t = 0; t < 4; ++t)
#pragma unroll
            for (int j = 0; j < 4; ++j) vv[t][j] = (aV[t][j] - mv4[t][j] * cV[0]) * rv4[t][j];
        const u32x4 v0 = widen(vv[0], vv[1]), v1 = widen(vv[2], vv[3]);
        __builtin_amdgcn_raw_buffer_store_b128(v0, rs, st_v, 0, 16); __builtin_amdgcn_raw_buffer_store_b128(v1, rs, st_v + 2048, 0, 16);
        WAITBAR(63);
        f32x4 aQ[4] = {}, cQ = {};
        auto rd_q = [&](int i, int u) { fb[i & 1][u][0] = BFRAG(QPIECE(i), u); SBAR(); };
        auto mq = [&](int i, int u, int t) { aQ[t] = MFMA16(fb[i & 1][u][0], fa[i & 1][u][t], aQ[t]); aQ[t + 1] = MFMA16(fb[i & 1][u][0], fa[i & 1][u][t + 1], aQ[t + 1]); SBAR(); };
        auto mcq = [&](int i) { cQ = MFMA16(fb[i & 1][0][0], ones4, cQ); cQ = MFMA16(fb[i & 1][1][0], ones4, cQ); PIN(cQ); SBAR(); };
#define Q_IT(i) do { rd_q(i, 0); rd_a(i, 0, 0); mq(i - 1, 0, 0); rd_a(i, 0, 2); mq(i - 1, 0, 2); rd_q(i, 1); mq(i - 1, 1, 0); rd_a(i, 1, 0); mq(i - 1, 1, 2); rd_a(i, 1, 2); mcq(i - 1); } while (0)
        rd_q(0, 0); rd_q(0, 1);
        *(LAS u32x4*)(L + xq + 2048) = kown;
        Q_IT(1); Q_IT(2); Q_IT(3); Q_IT(4);
        asm volatile("s_waitcnt vmcnt(0)" ::: "memory");
        ST32(tag, st_f);
        xv = ld2x8((const char*)x + xr_off); wv = ld2x8((const char*)wr + xw_off);
        Q_IT(5); Q_IT(6); Q_IT(7);
        mq(7, 0, 0); mq(7, 0, 2); mcq(7); mq(7, 1, 0); mq(7, 1, 2);
        f32x4 tq[4];
#pragma unroll
        for (int t = 0; t < 4; ++t)
#pragma unroll
            for (int j = 0; j < 4; ++j) tq[t][j] = (aQ[t][j] - mt[t] * cQ[j]) * (rt[t] * QSCALE);
        q0 = widen(tq[0], tq[1]); q1 = widen(tq[2], tq[3]);
        *(LAS u32x4*)(L + xq) = q0; *(LAS u32x4*)(L + xq + 1024) = q1;
        LBAR();
    }
    const f32x16 zero16 = {};
    float m0 = -INFINITY, m1 = -INFINITY;
    wsum = __uint_as_float(__builtin_amdgcn_readfirstlane(__float_as_uint(wsum)));
    asm volatile("" : "+s"(wsum)); PIN(xv); PIN(wv);
    u32x4 wdr0, wdr1, wdr2, wdr3;
    f32x16 o0, o1; unsigned peerf, peerf2;
    f32x4 pa, pb; float pl;
    unsigned redo = 0;
    _Pragma("nounroll") for (;;) {
        asm volatile(
            "global_load_dword v175, %[foff], %[wsb] sc1\n"
            "s_mov_b64 s[72:73], %[kvb]\n"
            "s_mov_b32 s80, 0xffff\n"
            "v_mfma_f32_32x32x16_bf16 v[176:191], %[kown], %[q0], 0\n"
            "s_mov_b32 s81, 0xffff\n"
            "s_mov_b32 s77, %[start]\n"
            "v_mov_b32 v224, %[one]\n"
            "v_mov_b32 v225, %[one]\n"
            "v_mfma_f32_32x32x16_bf16 v[192:207], %[kown], %[q1], 0\n"
            "s_add_u32 s72, s72, 4096\n"
            "s_addc_u32 s73, s73, 0\n"
            "s_add_u32 s66, s72, s77\n"
            "s_addc_u32 s67, s73, 0\n"
            "s_mov_b32 s78, 0x40000\n"
            "v_mov_b64 v[226:227], v[224:225]\n"
            "v_mov_b64 v[228:229], v[224:225]\n"
            "v_mov_b64 v[230:231], v[224:225]\n"
            "v_mov_b64 v[232:233], v[224:225]\n"
            "v_mov_b64 v[234:235], v[224:225]\n"
            "v_mov_b64 v[236:237], v[224:225]\n"
            "v_mov_b64 v[238:239], v[224:225]\n"
            "v_mov_b64 v[240:241], v[224:225]\n"
            "v_mov_b64 v[242:243], v[224:225]\n"
            "v_mov_b64 v[244:245], v[224:225]\n"
            "v_mov_b64 v[246:247], v[224:225]\n"
            "v_mov_b64 v[248:249], v[224:225]\n"
            "v_mov_b64 v[250:251], v[224:225]\n"
            "v_mov_b64 v[252:253], v[224:225]\n"
            "v_mov_b64 v[254:255], v[224:225]\n"
            "s_waitcnt lgkmcnt(0)\n"
            "s_barrier\n"
            ".Lat1_%=:\n"
            "s_waitcnt vmcnt(0)\n"
            "v_cmp_ne_u32 vcc, %[tag], v175\n"
            "s_cbranch_vccz .Lat2_%=\n"
            "global_load_dword v175, %[foff], %[wsb] sc1\n"
            "s_sub_u32 s78, s78, 1\n"
            "s_cbranch_scc0 .Lat1_%=\n"
            ".Lat2_%=:\n"
            "global_load_dwordx4 v[208:211], %[koff], s[66:67] offset:-4096 sc1\n"
            "s_mov_b64 exec, s[80:81]\n"
            "global_load_dwordx4 v[224:227], %[voff], s[66:67] offset:-3072 sc1\n"
            "global_load_dwordx4 v[228:231], %[voff], s[66:67] offset:-2560 sc1\n"
            "s_mov_b64 exec, -1\n"
            "global_load_dwordx4 v[212:215], %[koff], s[66:67] offset:-2048 sc1\n"
            "s_mov_b64 exec, s[80:81]\n"
            "global_load_dwordx4 v[232:235], %[voff], s[66:67] offset:-1024 sc1\n"
            "global_load_dwordx4 v[236:239], %[voff], s[66:67] offset:-512 sc1\n"
            "s_mov_b64 exec, -1\n"
            "global_load_dwordx4 v[216:219], %[koff], s[66:67] offset:0 sc1\n"
            "s_mov_b64 exec, s[80:81]\n"
            "global_load_dwordx4 v[240:243], %[voff], s[66:67] offset:1024 sc1\n"
            "global_load_dwordx4 v[244:247], %[voff], s[66:67] offset:1536 sc1\n"
            "s_mov_b64 exec, -1\n"
            "global_load_dwordx4 v[220:223], %[koff], s[66:67] offset:2048 sc1\n"
            "v_mov_b32 v141, %[tag]\n"
            "buffer_store_dword v141, %[clrf], %[rs], 0 offen sc1\n"
            "v_max3_f32 v176, v176, v177, %[m0]\n"
            "v_max3_f32 v192, v192, v193, %[m1]\n"
            "v_max3_f32 v176, v176, v178, v179\n"
            "v_max3_f32 v192, v192, v194, v195\n"
            "v_max3_f32 v176, v176, v180, v181\n"
            "v_max3_f32 v192, v192, v196, v197\n"
            "v_max3_f32 v176, v176, v182, v183\n"
            "v_max3_f32 v192, v192, v198, v199\n"
            "v_max3_f32 v176, v176, v184, v185\n"
            "v_max3_f32 v192, v192, v200, v201\n"
            "v_max3_f32 v176, v176, v186, v187\n"
            "v_max3_f32 v192, v192, v202, v203\n"
            "v_max3_f32 v176, v176, v188, v189\n"
            "v_max3_f32 v192, v192, v204, v205\n"
            "v_max3_f32 v176, v176, v190, v191\n"
            "v_max3_f32 v192, v192, v206, v207\n"
            "v_mov_b32 v177, v176\n"
            "v_mov_b32 v193, v192\n"
            "v_mov_b64 v[200:201], 0\n"
            "v_mov_b64 v[202:203], 0\n"
            "v_permlane32_swap_b32 v176, v177\n"
            "v_permlane32_swap_b32 v192, v193\n"
            "v_mfma_f32_32x32x16_bf16 %[o0], v[200:203], v[200:203], 0\n"
            "v_mov_b64 v[204:205], 0\n"
            "v_mov_b64 v[206:207], 0\n"
            "v_mov_b32 v174, 0xdddd\n"
            "v_min_f32 v142, -v176, -v177\n"
            "v_min_f32 v143, -v176, -v177\n"
            "v_min_f32 v158, -v192, -v193\n"
            "v_min_f32 v159, -v192, -v193\n"
            "v_mfma_f32_32x32x16_bf16 %[o1], v[200:203], v[200:203], 0\n"
            "v_mov_b64 v[144:145], v[142:143]\n"
            "v_mov_b64 v[146:147], v[142:143]\n"
            "v_mov_b64 v[148:149], v[142:143]\n"
            "v_mov_b64 v[150:151], v[142:143]\n"
            "v_mov_b64 v[152:153], v[142:143]\n"
            "v_mov_b64 v[154:155], v[142:143]\n"
            "v_mov_b64 v[156:157], v[142:143]\n"
            "v_mov_b64 v[160:161], v[158:159]\n"
            "v_mov_b64 v[162:163], v[158:159]\n"
            "v_mov_b64 v[164:165], v[158:159]\n"
            "v_mov_b64 v[166:167], v[158:159]\n"
            "v_mov_b64 v[168:169], v[158:159]\n"
            "v_mov_b64 v[170:171], v[158:159]\n"
            "v_mov_b64 v[172:173], v[158:159]\n"
            "s_waitcnt vmcnt(9)\n"
            "v_mfma_f32_32x32x16_bf16 v[176:191], v[208:211], %[q0], v[142:157]\n"
            "s_mov_b64 s[68:69], %[wo]\n"
            "s_mov_b64 s[70:71], %[wg]\n"
            "s_mov_b32 s76, %[ldsdst]\n"
            "s_mov_b32 s79, %[sched]\n"
            "s_mov_b32 s75, 0\n"
            "s_mov_b32 s74, 2\n"
            "s_setprio 3\n"
            "s_nop 7\n"
            ".Lat3_%=:\n"
            "s_mov_b64 s[64:65], s[66:67]\n"
            "s_add_u32 s77, s77, 0x2000\n"
            "s_and_b32 s77, s77, 0xffff\n"
            "s_add_u32 s66, s72, s77\n"
            "s_addc_u32 s67, s73, 0\n"
            "s_bitcmp1_b32 s79, 2\n"
            "s_cbranch_scc1 .Lat8_%=\n"
            ".Lat9_%=:\n"
            "s_lshr_b32 s79, s79, 4\n"
            "v_exp_f32 v176, v176\n"
            "v_exp_f32 v177, v177\n"
            "v_smfmac_f32_32x32x32_bf16 %[o1], v[252:255], v[200:207], v174\n"
            "v_exp_f32 v178, v178\n"
            "v_exp_f32 v179, v179\n"
            "v_exp_f32 v180, v180\n"
            "v_mfma_f32_32x32x16_bf16 v[192:207], v[208:211], %[q1], v[158:173]\n"
            "s_mov_b64 exec, s[80:81]\n"
            "global_load_dwordx4 v[248:251], %[voff], s[64:65] offset:3072 sc1\n"
            "global_load_dwordx4 v[252:255], %[voff], s[64:65] offset:3584 sc1\n"
            "s_mov_b64 exec, -1\n"
            "global_load_dwordx4 v[208:211], %[koff], s[66:67] offset:-4096 sc1\n"
            "s_waitcnt vmcnt(9)\n"
            "v_exp_f32 v181, v181\n"
            "v_exp_f32 v182, v182\n"
            "v_exp_f32 v183, v183\n"
            "v_exp_f32 v184, v184\n"
            "v_exp_f32 v185, v185\n"
            "v_exp_f32 v186, v186\n"
            "v_smfmac_f32_32x32x32_bf16 %[o0], v[224:227], v[176:183], v174\n"
            "v_exp_f32 v187, v187\n"
            "v_exp_f32 v188, v188\n"
            "v_exp_f32 v189, v189\n"
            "v_exp_f32 v190, v190\n"
            "v_exp_f32 v191, v191\n"
            "v_exp_f32 v192, v192\n"
            "v_exp_f32 v193, v193\n"
            "v_smfmac_f32_32x32x32_bf16 %[o0], v[228:231], v[184:191], v174\n"
            "v_exp_f32 v194, v194\n"
            "v_exp_f32 v195, v195\n"
            "v_exp_f32 v196, v196\n"
            "v_mfma_f32_32x32x16_bf16 v[176:191], v[212:215], %[q0], v[142:157]\n"
            "v_exp_f32 v197, v197\n"
            "v_exp_f32 v198, v198\n"
            "v_exp_f32 v199, v199\n"
            "v_exp_f32 v200, v200\n"
            "v_exp_f32 v201, v201\n"
            "v_exp_f32 v202, v202\n"
            "v_smfmac_f32_32x32x32_bf16 %[o1], v[224:227], v[192:199], v174\n"
            "v_exp_f32 v203, v203\n"
            "v_exp_f32 v204, v204\n"
            "v_exp_f32 v205, v205\n"
            "v_exp_f32 v206, v206\n"
            "v_exp_f32 v207, v207\n"
            "v_exp_f32 v176, v176\n"
            "v_exp_f32 v177, v177\n"
            "v_smfmac_f32_32x32x32_bf16 %[o1], v[228:231], v[200:207], v174\n"
            "v_exp_f32 v178, v178\n"
            "v_exp_f32 v179, v179\n"
            "v_exp_f32 v180, v180\n"
            "v_mfma_f32_32x32x16_bf16 v[192:207], v[212:215], %[q1], v[158:173]\n"
            "s_mov_b64 exec, s[80:81]\n"
            "global_load_dwordx4 v[224:227], %[voff], s[66:67] offset:-3072 sc1\n"
            "global_load_dwordx4 v[228:231], %[voff], s[66:67] offset:-2560 sc1\n"
            "s_mov_b64 exec, -1\n"
            "global_load_dwordx4 v[212:215], %[koff], s[66:67] offset:-2048 sc1\n"
            "s_waitcnt vmcnt(9)\n"
            "v_exp_f32 v181, v181\n"
            "v_exp_f32 v182, v182\n"
            "v_exp_f32 v183, v183\n"
            "v_exp_f32 v184, v184\n"
            "v_exp_f32 v185, v185\n"
            "v_exp_f32 v186, v186\n"
            "v_smfmac_f32_32x32x32_bf16 %[o0], v[232:235], v[176:183], v174\n"
            "v_exp_f32 v187, v187\n"
            "v_exp_f32 v188, v188\n"
            "v_exp_f32 v189, v189\n"
            "v_exp_f32 v190, v190\n"
            "v_exp_f32 v191, v191\n"
            "v_exp_f32 v192, v192\n"
            "v_exp_f32 v193, v193\n"
            "v_smfmac_f32_32x32x32_bf16 %[o0], v[236:239], v[184:191], v174\n"
            "v_exp_f32 v194, v194\n"
            "v_exp_f32 v195, v195\n"
            "v_exp_f32 v196, v196\n"
            "v_mfma_f32_32x32x16_bf16 v[176:191], v[216:219], %[q0], v[142:157]\n"
            "v_exp_f32 v197, v197\n"
            "v_exp_f32 v198, v198\n"
            "v_exp_f32 v199, v199\n"
            "v_exp_f32 v200, v200\n"
            "v_exp_f32 v201, v201\n"
            "v_exp_f32 v202, v202\n"
            "v_smfmac_f32_32x32x32_bf16 %[o1], v[232:235], v[192:199], v174\n"
            "v_exp_f32 v203, v203\n"
            "v_exp_f32 v204, v204\n"
            "v_exp_f32 v205, v205\n"
            "v_exp_f32 v206, v206\n"
            "v_exp_f32 v207, v207\n"
            "v_exp_f32 v176, v176\n"
            "v_exp_f32 v177, v177\n"
            "v_smfmac_f32_32x32x32_bf16 %[o1], v[236:239], v[200:207], v174\n"
            "v_exp_f32 v178, v178\n"
            "v_exp_f32 v179, v179\n"
            "v_exp_f32 v180, v180\n"
            "v_mfma_f32_32x32x16_bf16 v[192:207], v[216:219], %[q1], v[158:173]\n"
            "s_mov_b64 exec, s[80:81]\n"
            "global_load_dwordx4 v[232:235], %[voff], s[66:67] offset:-1024 sc1\n"
            "global_load_dwordx4 v[236:239], %[voff], s[66:67] offset:-512 sc1\n"
            "s_mov_b64 exec, -1\n"
            "global_load_dwordx4 v[216:219], %[koff], s[66:67] offset:0 sc1\n"
            "s_waitcnt vmcnt(9)\n"
            "v_exp_f32 v181, v181\n"
            "v_exp_f32 v182, v182\n"
            "v_exp_f32 v183, v183\n"
            "v_exp_f32 v184, v184\n"
            "v_exp_f32 v185, v185\n"
            "v_exp_f32 v186, v186\n"
            "v_smfmac_f32_32x32x32_bf16 %[o0], v[240:243], v[176:183], v174\n"
            "v_exp_f32 v187, v187\n"
            "v_exp_f32 v188, v188\n"
            "v_exp_f32 v189, v189\n"
            "v_exp_f32 v190, v190\n"
            "v_exp_f32 v191, v191\n"
            "v_exp_f32 v192, v192\n"
            "v_exp_f32 v193, v193\n"
            "v_smfmac_f32_32x32x32_bf16 %[o0], v[244:247], v[184:191], v174\n"
            "v_exp_f32 v194, v194\n"
            "v_exp_f32 v195, v195\n"
            "v_exp_f32 v196, v196\n"
            "v_mfma_f32_32x32x16_bf16 v[176:191], v[220:223], %[q0], v[142:157]\n"
            "v_exp_f32 v197, v197\n"
            "v_exp_f32 v198, v198\n"
            "v_exp_f32 v199, v199\n"
            "v_exp_f32 v200, v200\n"
            "v_exp_f32 v201, v201\n"
            "v_exp_f32 v202, v202\n"
            "v_smfmac_f32_32x32x32_bf16 %[o1], v[240:243], v[192:199], v174\n"
            "v_exp_f32 v203, v203\n"
            "v_exp_f32 v204, v204\n"
            "v_exp_f32 v205, v205\n"
            "v_exp_f32 v206, v206\n"
            "v_exp_f32 v207, v207\n"
            "v_exp_f32 v176, v176\n"
            "v_exp_f32 v177, v177\n"
            "v_smfmac_f32_32x32x32_bf16 %[o1], v[244:247], v[200:207], v174\n"
            "v_exp_f32 v178, v178\n"
            "v_exp_f32 v179, v179\n"
            "v_exp_f32 v180, v180\n"
            "v_mfma_f32_32x32x16_bf16 v[192:207], v[220:223], %[q1], v[158:173]\n"
            "s_mov_b64 exec, s[80:81]\n"
            "global_load_dwordx4 v[240:243], %[voff], s[66:67] offset:1024 sc1\n"
            "global_load_dwordx4 v[244:247], %[voff], s[66:67] offset:1536 sc1\n"
            "s_mov_b64 exec, -1\n"
            "global_load_dwordx4 v[220:223], %[koff], s[66:67] offset:2048 sc1\n"
            "s_waitcnt vmcnt(9)\n"
            "v_exp_f32 v181, v181\n"
            "v_exp_f32 v182, v182\n"
            "v_exp_f32 v183, v183\n"
            "v_exp_f32 v184, v184\n"
            "v_exp_f32 v185, v185\n"
            "v_exp_f32 v186, v186\n"
            "v_smfmac_f32_32x32x32_bf16 %[o0], v[248:251], v[176:183], v174\n"
            "v_exp_f32 v187, v187\n"
            "v_exp_f32 v188, v188\n"
            "v_exp_f32 v189, v189\n"
            "v_exp_f32 v190, v190\n"
            "v_exp_f32 v191, v191\n"
            "v_exp_f32 v192, v192\n"
            "v_exp_f32 v193, v193\n"
            "v_smfmac_f32_32x32x32_bf16 %[o0], v[252:255], v[184:191], v174\n"
            "v_exp_f32 v194, v194\n"
            "v_exp_f32 v195, v195\n"
            "v_exp_f32 v196, v196\n"
            "v_mfma_f32_32x32x16_bf16 v[176:191], v[208:211], %[q0], v[142:157]\n"
            "v_exp_f32 v197, v197\n"
            "v_exp_f32 v198, v198\n"
            "v_exp_f32 v199, v199\n"
            "v_exp_f32 v200, v200\n"
            "v_exp_f32 v201, v201\n"
            "v_exp_f32 v202, v202\n"
            "v_smfmac_f32_32x32x32_bf16 %[o1], v[248:251], v[192:199], v174\n"
            "v_exp_f32 v203, v203\n"
            "v_exp_f32 v204, v204\n"
            "v_exp_f32 v205, v205\n"
            "v_exp_f32 v206, v206\n"
            "v_exp_f32 v207, v207\n"
            "s_sub_u32 s74, s74, 1\n"
            "s_cbranch_scc0 .Lat3_%=\n"
            "s_mov_b32 s74, 3\n"
            "s_cmp_lg_u32 %[is1], 0\n"
            "s_cbranch_scc1 .Lat5_%=\n"
            ".Lat4_%=:\n"
            "s_mov_b64 s[64:65], s[66:67]\n"
            "s_add_u32 s77, s77, 0x2000\n"
            "s_and_b32 s77, s77, 0xffff\n"
            "s_add_u32 s66, s72, s77\n"
            "s_addc_u32 s67, s73, 0\n"
            "s_bitcmp1_b32 s79, 2\n"
            "s_cbranch_scc1 .Lat10_%=\n"
            ".Lat11_%=:\n"
            "s_lshr_b32 s79, s79, 4\n"
            "v_exp_f32 v176, v176\n"
            "v_exp_f32 v177, v177\n"
            "v_smfmac_f32_32x32x32_bf16 %[o1], v[252:255], v[200:207], v174\n"
            "v_exp_f32 v178, v178\n"
            "v_exp_f32 v179, v179\n"
            "v_exp_f32 v180, v180\n"
            "v_mfma_f32_32x32x16_bf16 v[192:207], v[208:211], %[q1], v[158:173]\n"
            "s_mov_b64 exec, s[80:81]\n"
            "global_load_dwordx4 v[248:251], %[voff], s[64:65] offset:3072 sc1\n"
            "global_load_dwordx4 v[252:255], %[voff], s[64:65] offset:3584 sc1\n"
            "s_mov_b64 exec, -1\n"
            "global_load_dwordx4 v[208:211], %[koff], s[66:67] offset:-4096 sc1\n"
            "s_waitcnt vmcnt(9)\n"
            "v_exp_f32 v181, v181\n"
            "v_exp_f32 v182, v182\n"
            "v_exp_f32 v183, v183\n"
            "v_exp_f32 v184, v184\n"
            "v_exp_f32 v185, v185\n"
            "v_exp_f32 v186, v186\n"
            "v_smfmac_f32_32x32x32_bf16 %[o0], v[224:227], v[176:183], v174\n"
            "v_exp_f32 v187, v187\n"
            "v_exp_f32 v188, v188\n"
            "v_exp_f32 v189, v189\n"
            "v_exp_f32 v190, v190\n"
            "v_exp_f32 v191, v191\n"
            "v_exp_f32 v192, v192\n"
            "v_exp_f32 v193, v193\n"
            "v_smfmac_f32_32x32x32_bf16 %[o0], v[228:231], v[184:191], v174\n"
            "v_exp_f32 v194, v194\n"
            "v_exp_f32 v195, v195\n"
            "v_exp_f32 v196, v196\n"
            "v_mfma_f32_32x32x16_bf16 v[176:191], v[212:215], %[q0], v[142:157]\n"
            "s_mov_b32 m0, s76\n"
            "s_add_u32 s76, s76, 0x2000\n"
            "global_load_lds_dwordx4 %[wooff], s[68:69]\n"
            "s_add_u32 s68, s68, 0x10000\n"
            "s_addc_u32 s69, s69, 0\n"
            "v_exp_f32 v197, v197\n"
            "v_exp_f32 v198, v198\n"
            "v_exp_f32 v199, v199\n"
            "v_exp_f32 v200, v200\n"
            "v_exp_f32 v201, v201\n"
            "v_exp_f32 v202, v202\n"
            "v_smfmac_f32_32x32x32_bf16 %[o1], v[224:227], v[192:199], v174\n"
            "v_exp_f32 v203, v203\n"
            "v_exp_f32 v204, v204\n"
            "v_exp_f32 v205, v205\n"
            "v_exp_f32 v206, v206\n"
            "v_exp_f32 v207, v207\n"
            "v_exp_f32 v176, v176\n"
            "v_exp_f32 v177, v177\n"
            "v_smfmac_f32_32x32x32_bf16 %[o1], v[228:231], v[200:207], v174\n"
            "v_exp_f32 v178, v178\n"
            "v_exp_f32 v179, v179\n"
            "v_exp_f32 v180, v180\n"
            "v_mfma_f32_32x32x16_bf16 v[192:207], v[212:215], %[q1], v[158:173]\n"
            "s_mov_b64 exec, s[80:81]\n"
            "global_load_dwordx4 v[224:227], %[voff], s[66:67] offset:-3072 sc1\n"
            "global_load_dwordx4 v[228:231], %[voff], s[66:67] offset:-2560 sc1\n"
            "s_mov_b64 exec, -1\n"
            "global_load_dwordx4 v[212:215], %[koff], s[66:67] offset:-2048 sc1\n"
            "s_waitcnt vmcnt(9)\n"
            "v_exp_f32 v181, v181\n"
            "v_exp_f32 v182, v182\n"
            "v_exp_f32 v183, v183\n"
            "v_exp_f32 v184, v184\n"
            "v_exp_f32 v185, v185\n"
            "v_exp_f32 v186, v186\n"
            "v_smfmac_f32_32x32x32_bf16 %[o0], v[232:235], v[176:183], v174\n"
            "v_exp_f32 v187, v187\n"
            "v_exp_f32 v188, v188\n"
            "v_exp_f32 v189, v189\n"
            "v_exp_f32 v190, v190\n"
            "v_exp_f32 v191, v191\n"
            "v_exp_f32 v192, v192\n"
            "v_exp_f32 v193, v193\n"
            "v_smfmac_f32_32x32x32_bf16 %[o0], v[236:239], v[184:191], v174\n"
            "v_exp_f32 v194, v194\n"
            "v_exp_f32 v195, v195\n"
            "v_exp_f32 v196, v196\n"
            "v_mfma_f32_32x32x16_bf16 v[176:191], v[216:219], %[q0], v[142:157]\n"
            "v_exp_f32 v197, v197\n"
            "v_exp_f32 v198, v198\n"
            "v_exp_f32 v199, v199\n"
            "v_exp_f32 v200, v200\n"
            "v_exp_f32 v201, v201\n"
            "v_exp_f32 v202, v202\n"
            "v_smfmac_f32_32x32x32_bf16 %[o1], v[232:235], v[192:199], v174\n"
            "v_exp_f32 v203, v203\n"
            "v_exp_f32 v204, v204\n"
            "v_exp_f32 v205, v205\n"
            "v_exp_f32 v206, v206\n"
            "v_exp_f32 v207, v207\n"
            "v_exp_f32 v176, v176\n"
            "v_exp_f32 v177, v177\n"
            "v_smfmac_f32_32x32x32_bf16 %[o1], v[236:239], v[200:207], v174\n"
            "v_exp_f32 v178, v178\n"
            "v_exp_f32 v179, v179\n"
            "v_exp_f32 v180, v180\n"
            "v_mfma_f32_32x32x16_bf16 v[192:207], v[216:219], %[q1], v[158:173]\n"
            "s_mov_b64 exec, s[80:81]\n"
            "global_load_dwordx4 v[232:235], %[voff], s[66:67] offset:-1024 sc1\n"
            "global_load_dwordx4 v[236:239], %[voff], s[66:67] offset:-512 sc1\n"
            "s_mov_b64 exec, -1\n"
            "global_load_dwordx4 v[216:219], %[koff], s[66:67] offset:0 sc1\n"
            "s_waitcnt vmcnt(9)\n"
            "v_exp_f32 v181, v181\n"
            "v_exp_f32 v182, v182\n"
            "v_exp_f32 v183, v183\n"
            "v_exp_f32 v184, v184\n"
            "v_exp_f32 v185, v185\n"
            "v_exp_f32 v186, v186\n"
            "v_smfmac_f32_32x32x32_bf16 %[o0], v[240:243], v[176:183], v174\n"
            "v_exp_f32 v187, v187\n"
            "v_exp_f32 v188, v188\n"
            "v_exp_f32 v189, v189\n"
            "v_exp_f32 v190, v190\n"
            "v_exp_f32 v191, v191\n"
            "v_exp_f32 v192, v192\n"
            "v_exp_f32 v193, v193\n"
            "v_smfmac_f32_32x32x32_bf16 %[o0], v[244:247], v[184:191], v174\n"
            "v_exp_f32 v194, v194\n"
            "v_exp_f32 v195, v195\n"
            "v_exp_f32 v196, v196\n"
            "v_mfma_f32_32x32x16_bf16 v[176:191], v[220:223], %[q0], v[142:157]\n"
            "s_mov_b32 m0, s76\n"
            "s_add_u32 s76, s76, 0x2000\n"
            "global_load_lds_dwordx4 %[wooff], s[68:69]\n"
            "s_add_u32 s68, s68, 0x10000\n"
            "s_addc_u32 s69, s69, 0\n"
            "v_exp_f32 v197, v197\n"
            "v_exp_f32 v198, v198\n"
            "v_exp_f32 v199, v199\n"
            "v_exp_f32 v200, v200\n"
            "v_exp_f32 v201, v201\n"
            "v_exp_f32 v202, v202\n"
            "v_smfmac_f32_32x32x32_bf16 %[o1], v[240:243], v[192:199], v174\n"
            "v_exp_f32 v203, v203\n"
            "v_exp_f32 v204, v204\n"
            "v_exp_f32 v205, v205\n"
            "v_exp_f32 v206, v206\n"
            "v_exp_f32 v207, v207\n"
            "v_exp_f32 v176, v176\n"
            "v_exp_f32 v177, v177\n"
            "v_smfmac_f32_32x32x32_bf16 %[o1], v[244:247], v[200:207], v174\n"
            "v_exp_f32 v178, v178\n"
            "v_exp_f32 v179, v179\n"
            "v_exp_f32 v180, v180\n"
            "v_mfma_f32_32x32x16_bf16 v[192:207], v[220:223], %[q1], v[158:173]\n"
            "s_mov_b64 exec, s[80:81]\n"
            "global_load_dwordx4 v[240:243], %[voff], s[66:67] offset:1024 sc1\n"
            "global_load_dwordx4 v[244:247], %[voff], s[66:67] offset:1536 sc1\n"
            "s_mov_b64 exec, -1\n"
            "global_load_dwordx4 v[220:223], %[koff], s[66:67] offset:2048 sc1\n"
            "s_waitcnt vmcnt(9)\n"
            "v_exp_f32 v181, v181\n"
            "v_exp_f32 v182, v182\n"
            "v_exp_f32 v183, v183\n"
            "v_exp_f32 v184, v184\n"
            "v_exp_f32 v185, v185\n"
            "v_exp_f32 v186, v186\n"
            "v_smfmac_f32_32x32x32_bf16 %[o0], v[248:251], v[176:183], v174\n"
            "v_exp_f32 v187, v187\n"
            "v_exp_f32 v188, v188\n"
            "v_exp_f32 v189, v189\n"
            "v_exp_f32 v190, v190\n"
            "v_exp_f32 v191, v191\n"
            "v_exp_f32 v192, v192\n"
            "v_exp_f32 v193, v193\n"
            "v_smfmac_f32_32x32x32_bf16 %[o0], v[252:255], v[184:191], v174\n"
            "v_exp_f32 v194, v194\n"
            "v_exp_f32 v195, v195\n"
            "v_exp_f32 v196, v196\n"
            "v_mfma_f32_32x32x16_bf16 v[176:191], v[208:211], %[q0], v[142:157]\n"
            "v_exp_f32 v197, v197\n"
            "v_exp_f32 v198, v198\n"
            "v_exp_f32 v199, v199\n"
            "v_exp_f32 v200, v200\n"
            "v_exp_f32 v201, v201\n"
            "v_exp_f32 v202, v202\n"
            "v_smfmac_f32_32x32x32_bf16 %[o1], v[248:251], v[192:199], v174\n"
            "v_exp_f32 v203, v203\n"
            "v_exp_f32 v204, v204\n"
            "v_exp_f32 v205, v205\n"
            "v_exp_f32 v206, v206\n"
            "v_exp_f32 v207, v207\n"
            "s_sub_u32 s74, s74, 1\n"
            "s_cbranch_scc0 .Lat4_%=\n"
            "s_branch .Lat6_%=\n"
            ".Lat5_%=:\n"
            "s_mov_b64 s[64:65], s[66:67]\n"
            "s_add_u32 s77, s77, 0x2000\n"
            "s_and_b32 s77, s77, 0xffff\n"
            "s_add_u32 s66, s72, s77\n"
            "s_addc_u32 s67, s73, 0\n"
            "s_bitcmp1_b32 s79, 2\n"
            "s_cbranch_scc1 .Lat12_%=\n"
            ".Lat13_%=:\n"
            "s_lshr_b32 s79, s79, 4\n"
            "v_exp_f32 v176, v176\n"
            "v_exp_f32 v177, v177\n"
            "v_smfmac_f32_32x32x32_bf16 %[o1], v[252:255], v[200:207], v174\n"
            "v_exp_f32 v178, v178\n"
            "v_exp_f32 v179, v179\n"
            "v_exp_f32 v180, v180\n"
            "v_mfma_f32_32x32x16_bf16 v[192:207], v[208:211], %[q1], v[158:173]\n"
            "s_mov_b64 exec, s[80:81]\n"
            "global_load_dwordx4 v[248:251], %[voff], s[64:65] offset:3072 sc1\n"
            "global_load_dwordx4 v[252:255], %[voff], s[64:65] offset:3584 sc1\n"
            "s_mov_b64 exec, -1\n"
            "global_load_dwordx4 v[208:211], %[koff], s[66:67] offset:-4096 sc1\n"
            "s_waitcnt vmcnt(9)\n"
            "v_exp_f32 v181, v181\n"
            "v_exp_f32 v182, v182\n"
            "v_exp_f32 v183, v183\n"
            "v_exp_f32 v184, v184\n"
            "v_exp_f32 v185, v185\n"
            "v_exp_f32 v186, v186\n"
            "v_smfmac_f32_32x32x32_bf16 %[o0], v[224:227], v[176:183], v174\n"
            "v_exp_f32 v187, v187\n"
            "v_exp_f32 v188, v188\n"
            "v_exp_f32 v189, v189\n"
            "v_exp_f32 v190, v190\n"
            "v_exp_f32 v191, v191\n"
            "v_exp_f32 v192, v192\n"
            "v_exp_f32 v193, v193\n"
            "v_smfmac_f32_32x32x32_bf16 %[o0], v[228:231], v[184:191], v174\n"
            "v_exp_f32 v194, v194\n"
            "v_exp_f32 v195, v195\n"
            "v_exp_f32 v196, v196\n"
            "v_mfma_f32_32x32x16_bf16 v[176:191], v[212:215], %[q0], v[142:157]\n"
            "s_mov_b32 m0, s76\n"
            "s_add_u32 s76, s76, 0x2000\n"
            "global_load_lds_dwordx4 %[wooff], s[68:69]\n"
            "s_add_u32 s68, s68, 0x10000\n"
            "s_addc_u32 s69, s69, 0\n"
            "s_add_u32 m0, s76, 0x12000\n"
            "s_nop 0\n"
            "global_load_lds_dwordx4 %[guoff], s[70:71]\n"
            "s_add_u32 s70, s70, 458752\n"
            "s_addc_u32 s71, s71, 0\n"
            "v_exp_f32 v197, v197\n"
            "v_exp_f32 v198, v198\n"
            "v_exp_f32 v199, v199\n"
            "v_exp_f32 v200, v200\n"
            "v_exp_f32 v201, v201\n"
            "v_exp_f32 v202, v202\n"
            "v_smfmac_f32_32x32x32_bf16 %[o1], v[224:227], v[192:199], v174\n"
            "v_exp_f32 v203, v203\n"
            "v_exp_f32 v204, v204\n"
            "v_exp_f32 v205, v205\n"
            "v_exp_f32 v206, v206\n"
            "v_exp_f32 v207, v207\n"
            "v_exp_f32 v176, v176\n"
            "v_exp_f32 v177, v177\n"
            "v_smfmac_f32_32x32x32_bf16 %[o1], v[228:231], v[200:207], v174\n"
            "v_exp_f32 v178, v178\n"
            "v_exp_f32 v179, v179\n"
            "v_exp_f32 v180, v180\n"
            "v_mfma_f32_32x32x16_bf16 v[192:207], v[212:215], %[q1], v[158:173]\n"
            "s_mov_b64 exec, s[80:81]\n"
            "global_load_dwordx4 v[224:227], %[voff], s[66:67] offset:-3072 sc1\n"
            "global_load_dwordx4 v[228:231], %[voff], s[66:67] offset:-2560 sc1\n"
            "s_mov_b64 exec, -1\n"
            "global_load_dwordx4 v[212:215], %[koff], s[66:67] offset:-2048 sc1\n"
            "s_waitcnt vmcnt(9)\n"
            "v_exp_f32 v181, v181\n"
            "v_exp_f32 v182, v182\n"
            "v_exp_f32 v183, v183\n"
            "v_exp_f32 v184, v184\n"
            "v_exp_f32 v185, v185\n"
            "v_exp_f32 v186, v186\n"
            "v_smfmac_f32_32x32x32_bf16 %[o0], v[232:235], v[176:183], v174\n"
            "v_exp_f32 v187, v187\n"
            "v_exp_f32 v188, v188\n"
            "v_exp_f32 v189, v189\n"
            "v_exp_f32 v190, v190\n"
            "v_exp_f32 v191, v191\n"
            "v_exp_f32 v192, v192\n"
            "v_exp_f32 v193, v193\n"
            "v_smfmac_f32_32x32x32_bf16 %[o0], v[236:239], v[184:191], v174\n"
            "v_exp_f32 v194, v194\n"
            "v_exp_f32 v195, v195\n"
            "v_exp_f32 v196, v196\n"
            "v_mfma_f32_32x32x16_bf16 v[176:191], v[216:219], %[q0], v[142:157]\n"
            "v_exp_f32 v197, v197\n"
            "v_exp_f32 v198, v198\n"
            "v_exp_f32 v199, v199\n"
            "v_exp_f32 v200, v200\n"
            "v_exp_f32 v201, v201\n"
            "v_exp_f32 v202, v202\n"
            "v_smfmac_f32_32x32x32_bf16 %[o1], v[232:235], v[192:199], v174\n"
            "v_exp_f32 v203, v203\n"
            "v_exp_f32 v204, v204\n"
            "v_exp_f32 v205, v205\n"
            "v_exp_f32 v206, v206\n"
            "v_exp_f32 v207, v207\n"
            "v_exp_f32 v176, v176\n"
            "v_exp_f32 v177, v177\n"
            "v_smfmac_f32_32x32x32_bf16 %[o1], v[236:239], v[200:207], v174\n"
            "v_exp_f32 v178, v178\n"
            "v_exp_f32 v179, v179\n"
            "v_exp_f32 v180, v180\n"
            "v_mfma_f32_32x32x16_bf16 v[192:207], v[216:219], %[q1], v[158:173]\n"
            "s_mov_b64 exec, s[80:81]\n"
            "global_load_dwordx4 v[232:235], %[voff], s[66:67] offset:-1024 sc1\n"
            "global_load_dwordx4 v[236:239], %[voff], s[66:67] offset:-512 sc1\n"
            "s_mov_b64 exec, -1\n"
            "global_load_dwordx4 v[216:219], %[koff], s[66:67] offset:0 sc1\n"
            "s_waitcnt vmcnt(9)\n"
            "v_exp_f32 v181, v181\n"
            "v_exp_f32 v182, v182\n"
            "v_exp_f32 v183, v183\n"
            "v_exp_f32 v184, v184\n"
            "v_exp_f32 v185, v185\n"
            "v_exp_f32 v186, v186\n"
            "v_smfmac_f32_32x32x32_bf16 %[o0], v[240:243], v[176:183], v174\n"
            "v_exp_f32 v187, v187\n"
            "v_exp_f32 v188, v188\n"
            "v_exp_f32 v189, v189\n"
            "v_exp_f32 v190, v190\n"
            "v_exp_f32 v191, v191\n"
            "v_exp_f32 v192, v192\n"
            "v_exp_f32 v193, v193\n"
            "v_smfmac_f32_32x32x32_bf16 %[o0], v[244:247], v[184:191], v174\n"
            "v_exp_f32 v194, v194\n"
            "v_exp_f32 v195, v195\n"
            "v_exp_f32 v196, v196\n"
            "v_mfma_f32_32x32x16_bf16 v[176:191], v[220:223], %[q0], v[142:157]\n"
            "s_mov_b32 m0, s76\n"
            "s_add_u32 s76, s76, 0x2000\n"
            "global_load_lds_dwordx4 %[wooff], s[68:69]\n"
            "s_add_u32 s68, s68, 0x10000\n"
            "s_addc_u32 s69, s69, 0\n"
            "s_add_u32 m0, s76, 0x12000\n"
            "s_nop 0\n"
            "global_load_lds_dwordx4 %[guoff], s[70:71]\n"
            "s_add_u32 s70, s70, 458752\n"
            "s_addc_u32 s71, s71, 0\n"
            "v_exp_f32 v197, v197\n"
            "v_exp_f32 v198, v198\n"
            "v_exp_f32 v199, v199\n"
            "v_exp_f32 v200, v200\n"
            "v_exp_f32 v201, v201\n"
            "v_exp_f32 v202, v202\n"
            "v_smfmac_f32_32x32x32_bf16 %[o1], v[240:243], v[192:199], v174\n"
            "v_exp_f32 v203, v203\n"
            "v_exp_f32 v204, v204\n"
            "v_exp_f32 v205, v205\n"
            "v_exp_f32 v206, v206\n"
            "v_exp_f32 v207, v207\n"
            "v_exp_f32 v176, v176\n"
            "v_exp_f32 v177, v177\n"
            "v_smfmac_f32_32x32x32_bf16 %[o1], v[244:247], v[200:207], v174\n"
            "v_exp_f32 v178, v178\n"
            "v_exp_f32 v179, v179\n"
            "v_exp_f32 v180, v180\n"
            "v_mfma_f32_32x32x16_bf16 v[192:207], v[220:223], %[q1], v[158:173]\n"
            "s_mov_b64 exec, s[80:81]\n"
            "global_load_dwordx4 v[240:243], %[voff], s[66:67] offset:1024 sc1\n"
            "global_load_dwordx4 v[244:247], %[voff], s[66:67] offset:1536 sc1\n"
            "s_mov_b64 exec, -1\n"
            "global_load_dwordx4 v[220:223], %[koff], s[66:67] offset:2048 sc1\n"
            "s_waitcnt vmcnt(9)\n"
            "v_exp_f32 v181, v181\n"
            "v_exp_f32 v182, v182\n"
            "v_exp_f32 v183, v183\n"
            "v_exp_f32 v184, v184\n"
            "v_exp_f32 v185, v185\n"
            "v_exp_f32 v186, v186\n"
            "v_smfmac_f32_32x32x32_bf16 %[o0], v[248:251], v[176:183], v174\n"
            "v_exp_f32 v187, v187\n"
            "v_exp_f32 v188, v188\n"
            "v_exp_f32 v189, v189\n"
            "v_exp_f32 v190, v190\n"
            "v_exp_f32 v191, v191\n"
            "v_exp_f32 v192, v192\n"
            "v_exp_f32 v193, v193\n"
            "v_smfmac_f32_32x32x32_bf16 %[o0], v[252:255], v[184:191], v174\n"
            "v_exp_f32 v194, v194\n"
            "v_exp_f32 v195, v195\n"
            "v_exp_f32 v196, v196\n"
            "v_mfma_f32_32x32x16_bf16 v[176:191], v[208:211], %[q0], v[142:157]\n"
            "v_exp_f32 v197, v197\n"
            "v_exp_f32 v198, v198\n"
            "v_exp_f32 v199, v199\n"
            "v_exp_f32 v200, v200\n"
            "v_exp_f32 v201, v201\n"
            "v_exp_f32 v202, v202\n"
            "v_smfmac_f32_32x32x32_bf16 %[o1], v[248:251], v[192:199], v174\n"
            "v_exp_f32 v203, v203\n"
            "v_exp_f32 v204, v204\n"
            "v_exp_f32 v205, v205\n"
            "v_exp_f32 v206, v206\n"
            "v_exp_f32 v207, v207\n"
            "s_cmp_eq_u32 s74, 2\n"
            "s_cselect_b64 s[70:71], %[wu], s[70:71]\n"
            "s_sub_u32 s74, s74, 1\n"
            "s_cbranch_scc0 .Lat5_%=\n"
            "s_mov_b64 s[70:71], %[wd]\n"
            "global_load_dwordx4 %[wd0], %[wdoff], s[70:71]\n"
            "s_add_u32 s70, s70, 0x2000\n"
            "s_addc_u32 s71, s71, 0\n"
            "global_load_dwordx4 %[wd1], %[wdoff], s[70:71]\n"
            "s_add_u32 s70, s70, 0x2000\n"
            "s_addc_u32 s71, s71, 0\n"
            "global_load_dwordx4 %[wd2], %[wdoff], s[70:71]\n"
            "s_add_u32 s70, s70, 0x2000\n"
            "s_addc_u32 s71, s71, 0\n"
            "global_load_dwordx4 %[wd3], %[wdoff], s[70:71]\n"
            "s_branch .Lat6_%=\n"
            ".Lat8_%=:\n"
            "s_and_b32 s78, s79, 3\n"
            "s_cmp_lg_u32 s78, 2\n"
            "s_cbranch_scc1 .Lat14_%=\n"
            "s_setprio 2\n"
            ".Lat14_%=:\n"
            "s_cmp_lg_u32 s78, 1\n"
            "s_cbranch_scc1 .Lat15_%=\n"
            "s_setprio 1\n"
            ".Lat15_%=:\n"
            "s_cmp_lg_u32 s78, 0\n"
            "s_cbranch_scc1 .Lat16_%=\n"
            "s_setprio 0\n"
            ".Lat16_%=:\n"
            "s_bitcmp0_b32 s79, 3\n"
            "s_cbranch_scc1 .Lat9_%=\n"
            "s_mov_b32 exec_lo, 0xff\n"
            "s_mov_b32 exec_hi, 0\n"
            "global_load_dword v175, %[tch], %[wg]\n"
            "s_mov_b32 exec_lo, 0xff0000\n"
            "s_mov_b32 exec_hi, 0\n"
            "global_load_dword v175, %[tch], %[wu]\n"
            "s_mov_b32 exec_lo, 0xff000000\n"
            "s_mov_b32 exec_hi, 0\n"
            "global_load_dword v175, %[tch], %[wd]\n"
            "s_mov_b64 exec, -1\n"
            "s_branch .Lat9_%=\n"
            ".Lat10_%=:\n"
            "s_and_b32 s78, s79, 3\n"
            "s_cmp_lg_u32 s78, 2\n"
            "s_cbranch_scc1 .Lat17_%=\n"
            "s_setprio 2\n"
            ".Lat17_%=:\n"
            "s_cmp_lg_u32 s78, 1\n"
            "s_cbranch_scc1 .Lat18_%=\n"
            "s_setprio 1\n"
            ".Lat18_%=:\n"
            "s_cmp_lg_u32 s78, 0\n"
            "s_cbranch_scc1 .Lat19_%=\n"
            "s_setprio 0\n"
            ".Lat19_%=:\n"
            "s_bitcmp0_b32 s79, 3\n"
            "s_cbranch_scc1 .Lat11_%=\n"
            "s_mov_b32 exec_lo, 0xff\n"
            "s_mov_b32 exec_hi, 0\n"
            "global_load_dword v175, %[tch], %[wg]\n"
            "s_mov_b32 exec_lo, 0xff0000\n"
            "s_mov_b32 exec_hi, 0\n"
            "global_load_dword v175, %[tch], %[wu]\n"
            "s_mov_b32 exec_lo, 0xff000000\n"
            "s_mov_b32 exec_hi, 0\n"
            "global_load_dword v175, %[tch], %[wd]\n"
            "s_mov_b64 exec, -1\n"
            "s_branch .Lat11_%=\n"
            ".Lat12_%=:\n"
            "s_and_b32 s78, s79, 3\n"
            "s_cmp_lg_u32 s78, 2\n"
            "s_cbranch_scc1 .Lat20_%=\n"
            "s_setprio 2\n"
            ".Lat20_%=:\n"
            "s_cmp_lg_u32 s78, 1\n"
            "s_cbranch_scc1 .Lat21_%=\n"
            "s_setprio 1\n"
            ".Lat21_%=:\n"
            "s_cmp_lg_u32 s78, 0\n"
            "s_cbranch_scc1 .Lat22_%=\n"
            "s_setprio 0\n"
            ".Lat22_%=:\n"
            "s_bitcmp0_b32 s79, 3\n"
            "s_cbranch_scc1 .Lat13_%=\n"
            "s_mov_b32 exec_lo, 0xff\n"
            "s_mov_b32 exec_hi, 0\n"
            "global_load_dword v175, %[tch], %[wg]\n"
            "s_mov_b32 exec_lo, 0xff0000\n"
            "s_mov_b32 exec_hi, 0\n"
            "global_load_dword v175, %[tch], %[wu]\n"
            "s_mov_b32 exec_lo, 0xff000000\n"
            "s_mov_b32 exec_hi, 0\n"
            "global_load_dword v175, %[tch], %[wd]\n"
            "s_mov_b64 exec, -1\n"
            "s_branch .Lat13_%=\n"
            ".Lat6_%=:\n"
            "s_mov_b64 s[64:65], s[66:67]\n"
            "s_add_u32 s77, s77, 0x2000\n"
            "s_and_b32 s77, s77, 0xffff\n"
            "s_add_u32 s66, s72, s77\n"
            "s_addc_u32 s67, s73, 0\n"
            "s_bitcmp1_b32 s79, 2\n"
            "s_cbranch_scc1 .Lat23_%=\n"
            ".Lat24_%=:\n"
            "s_lshr_b32 s79, s79, 4\n"
            "v_exp_f32 v176, v176\n"
            "v_exp_f32 v177, v177\n"
            "v_smfmac_f32_32x32x32_bf16 %[o1], v[252:255], v[200:207], v174\n"
            "v_exp_f32 v178, v178\n"
            "v_exp_f32 v179, v179\n"
            "v_exp_f32 v180, v180\n"
            "v_mfma_f32_32x32x16_bf16 v[192:207], v[208:211], %[q1], v[158:173]\n"
            "s_mov_b64 exec, s[80:81]\n"
            "global_load_dwordx4 v[248:251], %[voff], s[64:65] offset:3072 sc1\n"
            "global_load_dwordx4 v[252:255], %[voff], s[64:65] offset:3584 sc1\n"
            "s_mov_b64 exec, -1\n"
            "global_load_dwordx4 v[208:211], %[koff], s[66:67] offset:-4096 sc1\n"
            "s_waitcnt vmcnt(9)\n"
            "v_exp_f32 v181, v181\n"
            "v_exp_f32 v182, v182\n"
            "v_exp_f32 v183, v183\n"
            "v_exp_f32 v184, v184\n"
            "v_exp_f32 v185, v185\n"
            "v_exp_f32 v186, v186\n"
            "v_smfmac_f32_32x32x32_bf16 %[o0], v[224:227], v[176:183], v174\n"
            "v_exp_f32 v187, v187\n"
            "v_exp_f32 v188, v188\n"
            "v_exp_f32 v189, v189\n"
            "v_exp_f32 v190, v190\n"
            "v_exp_f32 v191, v191\n"
            "v_exp_f32 v192, v192\n"
            "v_exp_f32 v193, v193\n"
            "v_smfmac_f32_32x32x32_bf16 %[o0], v[228:231], v[184:191], v174\n"
            "v_exp_f32 v194, v194\n"
            "v_exp_f32 v195, v195\n"
            "v_exp_f32 v196, v196\n"
            "v_mfma_f32_32x32x16_bf16 v[176:191], v[212:215], %[q0], v[142:157]\n"
            "v_exp_f32 v197, v197\n"
            "v_exp_f32 v198, v198\n"
            "v_exp_f32 v199, v199\n"
            "v_exp_f32 v200, v200\n"
            "v_exp_f32 v201, v201\n"
            "v_exp_f32 v202, v202\n"
            "v_smfmac_f32_32x32x32_bf16 %[o1], v[224:227], v[192:199], v174\n"
            "v_exp_f32 v203, v203\n"
            "v_exp_f32 v204, v204\n"
            "v_exp_f32 v205, v205\n"
            "v_exp_f32 v206, v206\n"
            "v_exp_f32 v207, v207\n"
            "v_exp_f32 v176, v176\n"
            "v_exp_f32 v177, v177\n"
            "v_smfmac_f32_32x32x32_bf16 %[o1], v[228:231], v[200:207], v174\n"
            "v_exp_f32 v178, v178\n"
            "v_exp_f32 v179, v179\n"
            "v_exp_f32 v180, v180\n"
            "v_mfma_f32_32x32x16_bf16 v[192:207], v[212:215], %[q1], v[158:173]\n"
            "s_waitcnt vmcnt(6)\n"
            "global_load_dword %[pf], %[pfoff], %[wsb] sc1\n"
            "global_load_dword %[pf2], %[pfoff2], %[wsb] sc1\n"
            "v_exp_f32 v181, v181\n"
            "v_exp_f32 v182, v182\n"
            "v_exp_f32 v183, v183\n"
            "v_exp_f32 v184, v184\n"
            "v_exp_f32 v185, v185\n"
            "v_exp_f32 v186, v186\n"
            "v_smfmac_f32_32x32x32_bf16 %[o0], v[232:235], v[176:183], v174\n"
            "v_exp_f32 v187, v187\n"
            "v_exp_f32 v188, v188\n"
            "v_exp_f32 v189, v189\n"
            "v_exp_f32 v190, v190\n"
            "v_exp_f32 v191, v191\n"
            "v_exp_f32 v192, v192\n"
            "v_exp_f32 v193, v193\n"
            "v_smfmac_f32_32x32x32_bf16 %[o0], v[236:239], v[184:191], v174\n"
            "v_exp_f32 v194, v194\n"
            "v_exp_f32 v195, v195\n"
            "v_exp_f32 v196, v196\n"
            "v_mfma_f32_32x32x16_bf16 v[176:191], v[216:219], %[q0], v[142:157]\n"
            "v_exp_f32 v197, v197\n"
            "v_exp_f32 v198, v198\n"
            "v_exp_f32 v199, v199\n"
            "v_exp_f32 v200, v200\n"
            "v_exp_f32 v201, v201\n"
            "v_exp_f32 v202, v202\n"
            "v_smfmac_f32_32x32x32_bf16 %[o1], v[232:235], v[192:199], v174\n"
            "v_exp_f32 v203, v203\n"
            "v_exp_f32 v204, v204\n"
            "v_exp_f32 v205, v205\n"
            "v_exp_f32 v206, v206\n"
            "v_exp_f32 v207, v207\n"
            "v_exp_f32 v176, v176\n"
            "v_exp_f32 v177, v177\n"
            "v_smfmac_f32_32x32x32_bf16 %[o1], v[236:239], v[200:207], v174\n"
            "v_exp_f32 v178, v178\n"
            "v_exp_f32 v179, v179\n"
            "v_exp_f32 v180, v180\n"
            "v_mfma_f32_32x32x16_bf16 v[192:207], v[216:219], %[q1], v[158:173]\n"
            "s_waitcnt vmcnt(3)\n"
            "v_exp_f32 v181, v181\n"
            "v_exp_f32 v182, v182\n"
            "v_exp_f32 v183, v183\n"
            "v_exp_f32 v184, v184\n"
            "v_exp_f32 v185, v185\n"
            "v_exp_f32 v186, v186\n"
            "v_smfmac_f32_32x32x32_bf16 %[o0], v[240:243], v[176:183], v174\n"
            "v_exp_f32 v187, v187\n"
            "v_exp_f32 v188, v188\n"
            "v_exp_f32 v189, v189\n"
            "v_exp_f32 v190, v190\n"
            "v_exp_f32 v191, v191\n"
            "v_exp_f32 v192, v192\n"
            "v_exp_f32 v193, v193\n"
            "v_smfmac_f32_32x32x32_bf16 %[o0], v[244:247], v[184:191], v174\n"
            "v_exp_f32 v194, v194\n"
            "v_exp_f32 v195, v195\n"
            "v_exp_f32 v196, v196\n"
            "v_mfma_f32_32x32x16_bf16 v[176:191], v[220:223], %[q0], v[142:157]\n"
            "v_exp_f32 v197, v197\n"
            "v_exp_f32 v198, v198\n"
            "v_exp_f32 v199, v199\n"
            "v_exp_f32 v200, v200\n"
            "v_exp_f32 v201, v201\n"
            "v_exp_f32 v202, v202\n"
            "v_smfmac_f32_32x32x32_bf16 %[o1], v[240:243], v[192:199], v174\n"
            "v_exp_f32 v203, v203\n"
            "v_exp_f32 v204, v204\n"
            "v_exp_f32 v205, v205\n"
            "v_exp_f32 v206, v206\n"
            "v_exp_f32 v207, v207\n"
            "v_exp_f32 v176, v176\n"
            "v_exp_f32 v177, v177\n"
            "v_smfmac_f32_32x32x32_bf16 %[o1], v[244:247], v[200:207], v174\n"
            "v_exp_f32 v178, v178\n"
            "v_exp_f32 v179, v179\n"
            "v_exp_f32 v180, v180\n"
            "v_mfma_f32_32x32x16_bf16 v[192:207], v[220:223], %[q1], v[158:173]\n"
            "s_waitcnt vmcnt(0)\n"
            "v_exp_f32 v181, v181\n"
            "v_exp_f32 v182, v182\n"
            "v_exp_f32 v183, v183\n"
            "v_exp_f32 v184, v184\n"
            "v_exp_f32 v185, v185\n"
            "v_exp_f32 v186, v186\n"
            "v_smfmac_f32_32x32x32_bf16 %[o0], v[248:251], v[176:183], v174\n"
            "v_exp_f32 v187, v187\n"
            "v_exp_f32 v188, v188\n"
            "v_exp_f32 v189, v189\n"
            "v_exp_f32 v190, v190\n"
            "v_exp_f32 v191, v191\n"
            "v_exp_f32 v192, v192\n"
            "v_exp_f32 v193, v193\n"
            "v_smfmac_f32_32x32x32_bf16 %[o0], v[252:255], v[184:191], v174\n"
            "v_exp_f32 v194, v194\n"
            "v_exp_f32 v195, v195\n"
            "v_exp_f32 v196, v196\n"
            "v_mfma_f32_32x32x16_bf16 v[176:191], v[208:211], %[q0], v[142:157]\n"
            "v_exp_f32 v197, v197\n"
            "v_exp_f32 v198, v198\n"
            "v_exp_f32 v199, v199\n"
            "v_exp_f32 v200, v200\n"
            "v_exp_f32 v201, v201\n"
            "v_exp_f32 v202, v202\n"
            "v_smfmac_f32_32x32x32_bf16 %[o1], v[248:251], v[192:199], v174\n"
            "v_exp_f32 v203, v203\n"
            "v_exp_f32 v204, v204\n"
            "v_exp_f32 v205, v205\n"
            "v_exp_f32 v206, v206\n"
            "v_exp_f32 v207, v207\n"
            "s_nop 1\n"
            "v_smfmac_f32_32x32x32_bf16 %[o1], v[252:255], v[200:207], v174\n"
            "s_branch .Lat7_%=\n"
            ".Lat23_%=:\n"
            "s_and_b32 s78, s79, 3\n"
            "s_cmp_lg_u32 s78, 2\n"
            "s_cbranch_scc1 .Lat25_%=\n"
            "s_setprio 2\n"
            ".Lat25_%=:\n"
            "s_cmp_lg_u32 s78, 1\n"
            "s_cbranch_scc1 .Lat26_%=\n"
            "s_setprio 1\n"
            ".Lat26_%=:\n"
            "s_cmp_lg_u32 s78, 0\n"
            "s_cbranch_scc1 .Lat27_%=\n"
            "s_setprio 0\n"
            ".Lat27_%=:\n"
            "s_bitcmp0_b32 s79, 3\n"
            "s_cbranch_scc1 .Lat24_%=\n"
            "s_mov_b32 exec_lo, 0xff\n"
            "s_mov_b32 exec_hi, 0\n"
            "global_load_dword v175, %[tch], %[wg]\n"
            "s_mov_b32 exec_lo, 0xff0000\n"
            "s_mov_b32 exec_hi, 0\n"
            "global_load_dword v175, %[tch], %[wu]\n"
            "s_mov_b32 exec_lo, 0xff000000\n"
            "s_mov_b32 exec_hi, 0\n"
            "global_load_dword v175, %[tch], %[wd]\n"
            "s_mov_b64 exec, -1\n"
            "s_branch .Lat24_%=\n"
            ".Lat7_%=:\n"
            "s_setprio 0\n"
            "s_waitcnt vmcnt(0)\n"
            "s_nop 9\n"
            : [o0] "=&v"(o0), [o1] "=&v"(o1), [wd0] "=&v"(wdr0), [wd1] "=&v"(wdr1), [wd2] "=&v"(wdr2), [wd3] "=&v"(wdr3), [pf] "=&v"(peerf), [pf2] "=&v"(peerf2)
            : [m0] "v"(m0), [m1] "v"(m1), [kown] "v"(kown), [q0] "v"(q0), [q1] "v"(q1), [koff] "v"(koff), [voff] "v"(voff), [one] "v"(one), [wooff] "v"(wo_off), [guoff] "v"(gu_off), [wdoff] "v"(wd_off), [tch] "v"(tch), [foff] "v"(kvf_off), [pfoff] "v"(pf_off), [pfoff2] "v"(pf_off2), [clrf] "v"(clr_f),
              [kvb] "s"(kvb), [wo] "s"(wo), [wg] "s"(wg), [wu] "s"(wu), [wd] "s"(wd), [ldsdst] "s"(pf_dst), [is1] "s"(is1i), [sched] "s"(sched), [start] "s"(kv_start), [wsb] "s"(ws), [tag] "s"(tag), [rs] "s"(rs)
            : "memory", "scc", "vcc", "s64", "s65", "s66", "s67", "s68", "s69", "s70", "s71", "s72", "s73", "s74", "s75", "s76", "s77", "s78", "s79", "s80", "s81", "v141", "v142", "v143",
              "v144", "v145", "v146", "v147", "v148", "v149", "v150", "v151", "v152", "v153", "v154", "v155", "v156", "v157", "v158", "v159", "v160", "v161", "v162", "v163", "v164", "v165", "v166", "v167",
              "v168", "v169", "v170", "v171", "v172", "v173", "v174", "v175", "v176", "v177", "v178", "v179", "v180", "v181", "v182", "v183", "v184", "v185", "v186", "v187", "v188", "v189", "v190", "v191",
              "v192", "v193", "v194", "v195", "v196", "v197", "v198", "v199", "v200", "v201", "v202", "v203", "v204", "v205", "v206", "v207", "v208", "v209", "v210", "v211", "v212", "v213", "v214", "v215",
              "v216", "v217", "v218", "v219", "v220", "v221", "v222", "v223", "v224", "v225", "v226", "v227", "v228", "v229", "v230", "v231", "v232", "v233", "v234", "v235", "v236", "v237", "v238", "v239",
              "v240", "v241", "v242", "v243", "v244", "v245", "v246", "v247", "v248", "v249", "v250", "v251", "v252", "v253", "v254", "v255");
        *(LAS f32x4*)(L + mine) = (f32x4){o1[0], o1[1], o1[2], o1[3]}; *(LAS f32x4*)(L + mine + 1024) = (f32x4){o1[4], o1[5], o1[6], o1[7]}; *(LAS float*)(L + minel) = o1[8];
        float e0, e1, e2, e3;
#define ABSADD(d, a, b) asm("v_add_f32 %0, |%1|, |%2|" : "=v"(d) : "v"(a), "v"(b))
        ABSADD(e0, o0[0], o1[0]); ABSADD(e1, o0[1], o1[1]); ABSADD(e2, o0[2], o1[2]); ABSADD(e3, o0[3], o1[3]);
        ABSADD(e0, e0, o0[4]); ABSADD(e1, e1, o0[5]); ABSADD(e2, e2, o0[6]); ABSADD(e3, e3, o0[7]); ABSADD(e0, e0, o1[4]); ABSADD(e1, e1, o1[5]); ABSADD(e2, e2, o1[6]); ABSADD(e3, e3, o1[7]);
        if (!(o0[8] + o1[8] <= L_MAX && (e0 + e1) + (e2 + e3) < 1e38f)) arr[4] = 1u;
        LBAR();
        pa = *(LAS const f32x4*)(L + (mine ^ 8192)); pb = *(LAS const f32x4*)(L + (mine ^ 8192) + 1024); pl = *(LAS const float*)(L + (minel ^ 1024));
        if (__builtin_expect(__builtin_amdgcn_readfirstlane(arr[4]) <= redo, 1)) break;
        redo = 1;
        unsigned ko = koff; PIN(ko);
        _Pragma("nounroll") for (int t = 0; t < 64; ++t) {
            const u32x4 kf = LD128(WS_KV + head * KV_STRIDE + t * 2048 + ko);
            const f32x16 s0 = MFMA32(kf, q0, zero16), s1 = MFMA32(kf, q1, zero16);
            m0 = fmaxf(m0, max16(s0)); m1 = fmaxf(m1, max16(s1));
        }
        LBAR();
    }
    {
        const float rl = __builtin_amdgcn_rcpf(o0[8] + pl);
        const u32x4 ov = {nosent(pk_bf16((o0[0] + pa[0]) * rl, (o0[1] + pa[1]) * rl)), nosent(pk_bf16((o0[2] + pa[2]) * rl, (o0[3] + pa[3]) * rl)),
                          nosent(pk_bf16((o0[4] + pb[0]) * rl, (o0[5] + pb[1]) * rl)), nosent(pk_bf16((o0[6] + pb[2]) * rl, (o0[7] + pb[3]) * rl))};
        ST128(ov, o_off);
        CFENCE(); SBAR();
    }
    if (__builtin_expect(!__all(peerf == tag && peerf2 == tag), 0)) {
        _Pragma("nounroll") for (unsigned spins = 0; spins < SPIN_CAP; ++spins) {
            CFENCE();
            if (__all(LD32(pf_off) == tag && LD32(pf_off2) == tag)) break;
        }
    }
    u32x4 of[8];
    unsigned gofs[4];
#pragma unroll
    for (int u = 0; u < 4; ++u) gofs[u] = ((u + g3 + 1) & 3) * 262144;
    const unsigned of_hi = of_off + 131072;
#define LDO(a, b, so) "buffer_load_dwordx4 %" #a ", %8, %10, %" #so " offen sc1\n\tbuffer_load_dwordx4 %" #b ", %9, %10, %" #so " offen sc1\n"
    SBAR();
    {
    int l; asm volatile("v_mbcnt_lo_u32_b32 %0, -1, 0\n\tv_mbcnt_hi_u32_b32 %0, -1, %0" : "=v"(l));
    const int c = l & 15, g4 = l >> 4, r8 = l >> 3, s8 = l & 7, q4 = c >> 2, p4 = l & 3, brow = 8 * g4 + q4;
    const int row = 64 * rb3 + 16 * wl + c, colb = 64 * g3 + 32 * wh + 16 * (g4 & 1) + 8 * (g4 >> 1);
    u32x4 wf[8][4];
    {
        const int orow = (g4 >> 1) * 16 + 4 * (g4 & 1) + q4;
        const unsigned b0 = WO_L + (256 * wh + orow) * 128 + (p4 & 1) * 8 + ((((p4 >> 1) | (wh << 2)) ^ (((orow >> 1) & 3) << 1)) * 16);
#pragma unroll
        for (int u = 0; u < 4; ++u) {
            const unsigned bu = b0 + (gofs[u] >> 5);
#pragma unroll
            for (int e = 0; e < 2; ++e)
#pragma unroll
                for (int ct = 3; ct >= 0; --ct) wf[2 * u + e][ct] = tr_frag(L + (bu ^ (ct * 32)) + e * 4096, 1024);
        }
    }
    float xf[8];
#pragma unroll
    for (int i = 0; i < 4; ++i) { xf[2 * i] = bf_lo(xv[i]); xf[2 * i + 1] = bf_hi(xv[i]); PIN(xf[2 * i]); PIN(xf[2 * i + 1]); }
    unsigned st_ld = WS_ST + (rb3 * 4 + wl) * 2048 + l * 4, sxc = UP_L + 12288 + wl * 256 + l * 4;
    unsigned mine = SC_L + w * 2048 + l * 16, other = mine ^ 8192;
    PIN(st_ld); PIN(sxc); PIN(mine); PIN(other);
    SBAR();
    asm volatile(LDO(0, 1, 11) LDO(2, 3, 12) LDO(4, 5, 13) LDO(6, 7, 14)
                 : "=&v"(of[0]), "=&v"(of[1]), "=&v"(of[2]), "=&v"(of[3]), "=&v"(of[4]), "=&v"(of[5]), "=&v"(of[6]), "=&v"(of[7])
                 : "v"(of_off), "v"(of_hi), "s"(rs), "s"(gofs[0]), "s"(gofs[1]), "s"(gofs[2]), "s"(gofs[3]) : "memory");
    f32x4 acc[4] = {};
    {
        unsigned nd = 0;
#define MM_O(u) do { _Pragma("unroll") for (int ct = 3; ct >= 0; --ct) acc[ct] = MFMA16(wf[2 * u][ct], of[2 * u], acc[ct]); \
                     _Pragma("unroll") for (int ct = 3; ct >= 0; --ct) acc[ct] = MFMA16(wf[2 * u + 1][ct], of[2 * u + 1], acc[ct]); } while (0)
#define LDP(a, b, so) "buffer_load_dwordx4 %" #a ", %9, %11, %" #so " offen sc1\n\tbuffer_load_dwordx4 %" #b ", %10, %11, %" #so " offen sc1\n\t"
        _Pragma("nounroll") for (unsigned spins = 0; spins < SPIN_CAP; ++spins) {
            asm volatile("s_waitcnt vmcnt(0)" : "+v"(of[0]), "+v"(of[1]), "+v"(of[2]), "+v"(of[3]), "+v"(of[4]), "+v"(of[5]), "+v"(of[6]), "+v"(of[7]) :: "memory");
            unsigned mx[4], pend;
#pragma unroll
            for (int u = 0; u < 4; ++u) mx[u] = max(umax4(of[2 * u]), umax4(of[2 * u + 1]));
            const unsigned mall = max(umax3(mx[0], mx[1], mx[2]), mx[3]);
            asm volatile("s_mov_b32 %8, 0\n\tv_cmp_eq_u32 vcc, -1, %20\n\ts_cbranch_vccz 4f\n\t"
                         "v_cmp_eq_u32 vcc, -1, %16\n\ts_cbranch_vccz 1f\n\ts_bitset1_b32 %8, 0\n\t" LDP(0, 1, 12)
                         "1:\n\tv_cmp_eq_u32 vcc, -1, %17\n\ts_cbranch_vccz 2f\n\ts_bitset1_b32 %8, 1\n\t" LDP(2, 3, 13)
                         "2:\n\tv_cmp_eq_u32 vcc, -1, %18\n\ts_cbranch_vccz 3f\n\ts_bitset1_b32 %8, 2\n\t" LDP(4, 5, 14)
                         "3:\n\tv_cmp_eq_u32 vcc, -1, %19\n\ts_cbranch_vccz 4f\n\ts_bitset1_b32 %8, 3\n\t" LDP(6, 7, 15) "4:"
                         : "+v"(of[0]), "+v"(of[1]), "+v"(of[2]), "+v"(of[3]), "+v"(of[4]), "+v"(of[5]), "+v"(of[6]), "+v"(of[7]), "=&s"(pend)
                         : "v"(of_off), "v"(of_hi), "s"(rs), "s"(gofs[0]), "s"(gofs[1]), "s"(gofs[2]), "s"(gofs[3]), "v"(mx[0]), "v"(mx[1]), "v"(mx[2]), "v"(mx[3]), "v"(mall) : "memory", "vcc");
            unsigned upto, run;
            asm volatile("s_add_u32 %0, %2, -1\n\ts_andn2_b32 %0, %0, %2\n\ts_andn2_b32 %1, %0, %3" : "=&s"(upto), "=s"(run) : "s"(pend), "s"(nd) : "scc");
            if (run & 1u) MM_O(0);
            if (run & 2u) MM_O(1);
            if (run & 4u) MM_O(2);
            if (run & 8u) { MM_O(3); break; }
            nd = upto;
        }
        asm volatile("s_waitcnt vmcnt(0)" :: "v"(of[0]), "v"(of[1]), "v"(of[2]), "v"(of[3]), "v"(of[4]), "v"(of[5]), "v"(of[6]), "v"(of[7]) : "memory");
    }
    *(LAS f32x4*)(L + other) = acc[2]; *(LAS f32x4*)(L + other + 1024) = acc[3];
    LBAR();
    u32x4 hp;
    {
        const f32x4 p0 = *(LAS const f32x4*)(L + mine) + acc[0], p1 = *(LAS const f32x4*)(L + mine + 1024) + acc[1];
        const unsigned a0 = pk_keep(p0[0], p0[1]), a1 = pk_keep(p0[2], p0[3]), a2 = pk_keep(p1[0], p1[1]), a3 = pk_keep(p1[2], p1[3]);
        hp = (u32x4){pk_bf16(xf[0] + bf_lo(a0), xf[1] + bf_hi(a0)), pk_bf16(xf[2] + bf_lo(a1), xf[3] + bf_hi(a1)), pk_bf16(xf[4] + bf_lo(a2), xf[5] + bf_hi(a2)), pk_bf16(xf[6] + bf_lo(a3), xf[7] + bf_hi(a3))};
        float s1 = 0.f, s2 = 0.f, s3 = 0.f;
#pragma unroll
        for (int i = 0; i < 4; ++i) { s1 = dot2(hp[i], 0x3F803F80u, s1); s2 = dot2(hp[i], hp[i], s2); s3 = dot2(hp[i], wv[i], s3); }
        auto u = __builtin_amdgcn_permlane16_swap(__float_as_uint(s1), __float_as_uint(s2), false, false);
        auto v = __builtin_amdgcn_permlane16_swap(__float_as_uint(s3), __float_as_uint(s3), false, false);
        auto t = __builtin_amdgcn_permlane32_swap(__float_as_uint(__uint_as_float(u[0]) + __uint_as_float(u[1])), __float_as_uint(__uint_as_float(v[0]) + __uint_as_float(v[1])), false, false);
        const float sv = __uint_as_float(t[0]) + __uint_as_float(t[1]);
        if (wh) *(LAS float*)(L + sxc) = sv;
        LBAR();
        if (!wh) __builtin_amdgcn_raw_buffer_store_b32(nosent(__float_as_uint(sv + *(LAS const float*)(L + sxc))), rs, st_ld, g3 * 256, 16);
        CFENCE(); SBAR();
    }
    unsigned coff = (rb3 * 4 + wl) * 1024 + colb * 2, ooff = (row * DM + colb) * 2, rowkey = 2047 - row, key_st = (g3 == 0 && g4 == 0) ? WS_CKEY + rb3 * 128 + wl * 8 : WS_END;
    PIN(coff); PIN(ooff); PIN(rowkey); PIN(key_st);
    float h[8];
#pragma unroll
    for (int i = 0; i < 4; ++i) { h[2 * i] = bf_lo(hp[i]); h[2 * i + 1] = bf_hi(hp[i]); PIN(h[2 * i]); PIN(h[2 * i + 1]); hp[i] = nosent(hp[i]); }
    hp = widen_pk(hp); PIN(hp);
    float mb, rstd; bool best;
    const unsigned key_off = WS_CKEY + (l >> 1) * 128 + (l & 1) * 16;
    unsigned cur = 0;
    if (is1 && w == 4) { __builtin_amdgcn_s_sleep(6); const u32x4 kk = LD128(key_off); cur = best_key(kk); }
    if (!wh) {
        unsigned v[8];
        _Pragma("nounroll") for (unsigned spins = 0; spins < SPIN_CAP; ++spins) {
            CFENCE();
#pragma unroll
            for (int k = 0; k < 8; ++k) v[k] = LD32(st_ld + k * 256);
            if (__all(umax3(umax3(v[0], v[1], v[2]), umax3(v[3], v[4], v[5]), max(v[6], v[7])) != SENT)) break;
        }
        const float tt = ((__uint_as_float(v[0]) + __uint_as_float(v[1])) + (__uint_as_float(v[2]) + __uint_as_float(v[3]))) + ((__uint_as_float(v[4]) + __uint_as_float(v[5])) + (__uint_as_float(v[6]) + __uint_as_float(v[7])));
        auto a = __builtin_amdgcn_permlane32_swap(__float_as_uint(tt), __float_as_uint(tt), false, false);
        auto b = __builtin_amdgcn_permlane16_swap(a[0], a[0], false, false);
        ln_scalars(__uint_as_float(b[0]), __uint_as_float(b[1]), mb, rstd);
        const float rr = (__uint_as_float(a[1]) - mb * wsum) * rstd;
        const unsigned ru = __float_as_uint(rr);
        const unsigned key = ((rr == rr ? (ru ^ (((int)ru >> 31) | 0x80000000u)) : 0u) & ~2047u) | rowkey;
        best = row_umax(key) == key;
        if (best) ST64(((u32x2){key, nosent(ru)}), key_st);
        *(LAS float*)(L + sxc) = g4 == 0 ? mb : g4 == 1 ? rstd : best ? 1.f : 0.f;
    }
    LBAR();
    if (wh) { LAS const float* p = (LAS const float*)(L + UP_L + 12288 + wl * 256) + c; mb = p[0]; rstd = p[16]; best = p[32] != 0.f; }
    {
        u32x4 lnp, op;
#pragma unroll
        for (int i = 0; i < 4; ++i) {
            const unsigned d = pk_keep(h[2 * i] - mb, h[2 * i + 1] - mb);
            lnp[i] = pk_bf16(bf_lo(d) * rstd, bf_hi(d) * rstd);
        }
        const u32x4 lw = widen_pk((u32x4){nosent(lnp[0]), nosent(lnp[1]), nosent(lnp[2]), nosent(lnp[3])});
        if (best) ST128(lw, WS_CLN + coff);
        CFENCE(); SBAR();
#pragma unroll
        for (int i = 0; i < 4; ++i) op[i] = pk_bf16(h[2 * i] + bf_lo(lnp[i]), h[2 * i + 1] + bf_hi(lnp[i]));
        const __amdgpu_buffer_rsrc_t ro = __builtin_amdgcn_make_buffer_rsrc(out, 0, SEQ * DM * 2, 0x00020000);
        __builtin_amdgcn_raw_buffer_store_b128(widen_pk(op), ro, ooff, 0, 16);
    }
    CFENCE(); SBAR();
    u32x4 kk = {}, xr = {};
    if (is1 && w == 4) { kk = LD128(key_off); xr = __builtin_amdgcn_raw_buffer_load_b128(rs, l * 16, WS_CLN + ((2047 - (cur & 2047u)) >> 4) * 1024, 16); }
    u32x4 ff[8], df[4];
    if (is1) {
        LAS char* stg = L + WO_L + w * 4096;
        *(LAS u32x4*)(stg + l * 16) = wdr0; *(LAS u32x4*)(stg + 1024 + l * 16) = wdr1; *(LAS u32x4*)(stg + 2048 + l * 16) = wdr2; *(LAS u32x4*)(stg + 3072 + l * 16) = wdr3;
        CFENCE();
        LAS const char* bp = L + FF_L + (64 * w + brow) * 64 + p4 * 8;
#pragma unroll
        for (int ct = 0; ct < 4; ++ct) {
            ff[ct] = tr_frag(bp + (ct >> 1) * 32768 + (ct & 1) * 32, 256); ff[4 + ct] = tr_frag(bp + (ct >> 1) * 32768 + (ct & 1) * 32 + 2048, 256);
            df[ct] = tr_frag(stg + brow * 128 + ct * 32 + p4 * 8, 512);
        }
    }
    asm volatile("s_waitcnt vmcnt(0)" ::: "memory");
    if (best && !(is1 && w == 4)) ST128(hp, WS_CH + coff);
    if (!(is1 || (is2 && w == 4))) return;
    if (is1) {
        LAS char* xrow = L + UP_L; LAS float* fred = (LAS float*)(L + UP_L + 1024);
        if (w == 4) {
            _Pragma("nounroll") for (unsigned spins = 0; spins < SPIN_CAP; ++spins) {
                if (__all(max(kk[0], kk[2]) <= cur && umax4(xr) != SENT)) break;
                cur = best_key(kk);
                CFENCE();
                kk = LD128(key_off);
                xr = __builtin_amdgcn_raw_buffer_load_b128(rs, l * 16, WS_CLN + ((2047 - (cur & 2047u)) >> 4) * 1024, 16);
            }
            *(LAS u32x4*)(xrow + l * 16) = xr;
            if (best) ST128(hp, WS_CH + coff);
        }
        LBAR();
        const u32x4 x0 = *(LAS const u32x4*)(xrow + w * 128 + g4 * 16), x1 = *(LAS const u32x4*)(xrow + w * 128 + 64 + g4 * 16);
        f32x4 a[4] = {};
#pragma unroll
        for (int ct = 0; ct < 4; ++ct) a[ct] = MFMA16(x0, ff[ct], a[ct]);
#pragma unroll
        for (int ct = 0; ct < 4; ++ct) a[ct] = MFMA16(x1, ff[4 + ct], a[ct]);
        fred[l * 8 + w] = sel4(g4, a[0][0], a[1][0], a[2][0], a[3][0]);
        LBAR();
        const f32x4 f0 = *(LAS const f32x4*)(fred + l * 8), f1 = *(LAS const f32x4*)(fred + l * 8 + 4);
        const float gu = rbf(((f0[0] + f0[1]) + (f0[2] + f0[3])) + ((f1[0] + f1[1]) + (f1[2] + f1[3])));
        auto sw = __builtin_amdgcn_permlane32_swap(__float_as_uint(gu), __float_as_uint(gu), false, false);
        const float gg = __uint_as_float(sw[0]), uu = __uint_as_float(sw[1]);
        const float av = rbf(gg * __builtin_amdgcn_rcpf(1.f + __builtin_amdgcn_exp2f(gg * -1.4426950408889634f))) * uu;
        LAS u16* ast = (LAS u16*)(L + UP_L + 3072 + w * 128);
        ast[l & 31] = (u16)pk_bf16(av, av);
        CFENCE();
        const u32x4 ax = *(LAS const u32x4*)(ast + g4 * 8);
        const f32x4 z = {};
        const f32x4 y0 = MFMA16(ax, df[0], z), y1 = MFMA16(ax, df[1], z), y2 = MFMA16(ax, df[2], z), y3 = MFMA16(ax, df[3], z);
        ST32(nosent(__float_as_uint(sel4(g4, y0[0], y1[0], y2[0], y3[0]))), WS_YP + fid * 2048 + w * 256 + l * 4);
    } else {
        const int col0 = (bid - FFN2_BASE) * 32;
        _Pragma("nounroll") for (unsigned spins = 0; spins < SPIN_CAP; ++spins) {
            CFENCE();
            kk = LD128(key_off);
            if (__all(umax4(kk) != SENT)) break;
            __builtin_amdgcn_s_sleep(2);
        }
        const unsigned nb = wave_umax(max(kk[0], kk[2]));
        const float rsel = __uint_as_float(wave_umax(kk[0] == nb ? kk[1] : kk[2] == nb ? kk[3] : 0u));
        const int fcol = 4 * s8 + ((l >> 4) & 1) + 2 * (l >> 5);
        const unsigned co = ((2047 - (nb & 2047u)) >> 4) * 1024 + (col0 + (fcol & ~1)) * 2;
        unsigned lnv, hv;
        _Pragma("nounroll") for (unsigned spins = 0; spins < SPIN_CAP; ++spins) {
            CFENCE();
            lnv = LD32(WS_CLN + co); hv = LD32(WS_CH + co);
            if (__all(max(lnv, hv) != SENT)) break;
        }
        float sig = rbf(__builtin_amdgcn_rcpf(1.f + __builtin_amdgcn_exp2f(rbf(rsel) * -1.4426950408889634f)));
        float ln = (fcol & 1) ? bf_hi(lnv) : bf_lo(lnv), hh = (fcol & 1) ? bf_hi(hv) : bf_lo(hv);
        unsigned fo = (l & 8) ? SEQ * DM * 2 : ((2047 - (nb & 2047u)) * DM + col0 + fcol) * 2;
        PIN(sig); PIN(ln); PIN(hh); PIN(fo);
        u32x4 yp[7];
        _Pragma("nounroll") for (unsigned spins = 0; spins < SPIN_CAP; ++spins) {
            CFENCE();
            unsigned mxu = 0;
#pragma unroll
            for (int s = 0; s < 7; ++s) yp[s] = __builtin_amdgcn_raw_buffer_load_b128(rs, WS_YP + r8 * 2048 + col0 * 4 + s8 * 16, s * 16384, 16);
#pragma unroll
            for (int s = 0; s < 7; ++s) mxu = max(mxu, umax4(yp[s]));
            if (__all(mxu != SENT)) break;
        }
        const f32x4 ys = ((__builtin_bit_cast(f32x4, yp[0]) + __builtin_bit_cast(f32x4, yp[1])) + (__builtin_bit_cast(f32x4, yp[2]) + __builtin_bit_cast(f32x4, yp[3]))) + ((__builtin_bit_cast(f32x4, yp[4]) + __builtin_bit_cast(f32x4, yp[5])) + __builtin_bit_cast(f32x4, yp[6]));
        auto u = __builtin_amdgcn_permlane16_swap(__float_as_uint(ys[0]), __float_as_uint(ys[1]), false, false);
        auto v = __builtin_amdgcn_permlane16_swap(__float_as_uint(ys[2]), __float_as_uint(ys[3]), false, false);
        auto t = __builtin_amdgcn_permlane32_swap(__float_as_uint(__uint_as_float(u[0]) + __uint_as_float(u[1])), __float_as_uint(__uint_as_float(v[0]) + __uint_as_float(v[1])), false, false);
        float y = __uint_as_float(t[0]) + __uint_as_float(t[1]);
        y += __int_as_float(DPP(__float_as_int(y), 0x128));
        const float res = hh + rbf(ln + rbf(rbf(y) * sig));
        const __amdgpu_buffer_rsrc_t ro = __builtin_amdgcn_make_buffer_rsrc(out, 0, SEQ * DM * 2, 0x00020000);
        __builtin_amdgcn_raw_buffer_store_b16((u16)pk_bf16(res, res), ro, fo, 0, 16);
        if (bid == FFN2_BASE && l == 0) ST32(tag, WS_EPOCH);
    }
    }
}

extern "C" void kernel_launch(void* const* d_in, const int* in_sizes, int n_in, void* d_out, int out_size, void* d_ws, size_t ws_size, hipStream_t stream) {
    (void)hipFuncSetAttribute((const void*)k_fused, hipFuncAttributeMaxDynamicSharedMemorySize, LDS_BYTES);
    k_fused<<<256, 512, LDS_BYTES, stream>>>((const u16*)d_in[0], (const u16*)d_in[1], (const u16*)d_in[2], (const u16*)d_in[4], (const u16*)d_in[5], (const u16*)d_in[6], (const u16*)d_in[7],
                                             (u16*)d_out, (char*)d_ws);
}
```
